```python
import torch

def _softmax(x, axis=-1):
    return torch.softmax(x, dim=axis)
def _gelu(x):
    return torch.nn.functional.gelu(x, approximate="tanh")
def _silu(x):
    return torch.nn.functional.silu(x)
def _sigmoid(x):
    return torch.sigmoid(x)
def _layernorm(x, axis=-1, eps=1e-5):
    m = x.mean(dim=axis, keepdim=True)
    v = x.var(dim=axis, keepdim=True, unbiased=False)
    return (x - m) / torch.sqrt(v + eps)
def _scatter_add(tgt, idx, src, axis=0):
    return tgt.clone().index_add_(axis, idx, src)
def _segment_sum(src, seg, n):
    out = torch.zeros((n,) + src.shape[1:], dtype=src.dtype)
    return out.index_add_(0, seg, src)
def _pad(x, pw):
    flat = tuple(v for pair in reversed(pw) for v in pair)
    return torch.nn.functional.pad(x, flat)

kernel_name = 'sequential_preLN__ssm_conv_h3__geglu_ffn__prompt'

def setup_inputs():

    _w = (lambda a: (lambda t: t.to(torch.bfloat16) if t.is_floating_point() else t)(torch.as_tensor(a)))
    def _A_setup():
        B, S, D = 1, 2048, 448
        ks = 4
        return {
            "q": _w((lambda *s: torch.randn(*s, dtype=torch.bfloat16) * 0.05)(B, S, D)),
            "k": _w((lambda *s: torch.randn(*s, dtype=torch.bfloat16) * 0.4)(B, S, D)),
            "v": _w((lambda *s: torch.randn(*s, dtype=torch.bfloat16) * 0.4)(B, S, D)),
            "shift_k": _w((lambda *s: torch.randn(*s, dtype=torch.bfloat16) * 0.4)(D, ks) * 0.5),
            "ssm_k": _w((lambda *s: torch.randn(*s, dtype=torch.bfloat16) * 0.4)(D, S) * (1.0 / S ** 0.5)),
            "ssm_D": _w((lambda *s: torch.randn(*s, dtype=torch.bfloat16) * 0.4)(D)),
        }
    _A_in = _A_setup()

    def _B_setup():
        B, S, D, F = 1, 2048, 448, 1792
        return {
            "x": _w((lambda *s: torch.randn(*s, dtype=torch.bfloat16) * 0.05)(B, S, D)),
            "w_gate": _w((lambda *s: torch.randn(*s, dtype=torch.bfloat16) * 0.05)(D, F)),
            "w_up": _w((lambda *s: torch.randn(*s, dtype=torch.bfloat16) * 0.05)(D, F)),
            "w_down": _w((lambda *s: torch.randn(*s, dtype=torch.bfloat16) * 0.05)(F, D)),
        }
    _B_in = _B_setup()

    return {
        "A_q": _A_in["q"],
        "A_k": _A_in["k"],
        "A_v": _A_in["v"],
        "A_shift_k": _A_in["shift_k"],
        "A_ssm_k": _A_in["ssm_k"],
        "A_ssm_D": _A_in["ssm_D"],
        "B_x": _B_in["x"],
        "B_w_gate": _B_in["w_gate"],
        "B_w_up": _B_in["w_up"],
        "B_w_down": _B_in["w_down"],
    }

def reference(A_q, A_k, A_v, A_shift_k, A_ssm_k, A_ssm_D, B_x, B_w_gate, B_w_up, B_w_down):
    def _A_ref(q, k, v, shift_k, ssm_k, ssm_D):
        B, S, D = q.shape
        ks = shift_k.shape[-1]
        k_pad = _pad(k, ((0, 0), (ks - 1, 0), (0, 0)))
        k_shift = torch.zeros_like(k)
        for t in range(ks):
            k_shift = k_shift + k_pad[:, t:t + S, :] * shift_k[:, ks - 1 - t][None, None, :]
        kv = k_shift * v
        L = 2 * S
        kv_pad = _pad(kv, ((0, 0), (0, S), (0, 0)))
        kf_pad = _pad(ssm_k, ((0, 0), (0, S)))
        kv_f = (lambda x, axes=(-2,-1): torch.fft.rfft2(x.float(), dim=axes))(kv_pad[:, None, :, :], axes=(1, 2))
        kf_f = (lambda x, axes=(-2,-1): torch.fft.rfft2(x.float(), dim=axes))(kf_pad[:, None, :], axes=(1, 2))
        kf_f = (lambda x, *d: x.permute(*d))(kf_f, 1, 2, 0)
        y_f = kv_f * kf_f[None, :, :, :]
        y = (lambda x, s, axes=(-2,-1): torch.fft.irfft2(x, s=s, dim=axes).to(torch.bfloat16))(y_f, (1, L), axes=(1, 2))[:, 0, :S, :]
        y = y + kv * ssm_D[None, None, :]
        return q * y

    def _B_ref(x, w_gate, w_up, w_down):
        return (_gelu(x @ w_gate) * (x @ w_up)) @ w_down

    x = A_q
    h = x + _A_ref(_layernorm(x, axis=-1), A_k, A_v, A_shift_k, A_ssm_k, A_ssm_D)
    h2 = h + _B_ref(_layernorm(h, axis=-1), B_w_gate, B_w_up, B_w_down)
    return h2
```

```cpp
#include <hip/hip_runtime.h>
#include <hip/hip_bf16.h>
#include <stdint.h>

typedef unsigned short u16;
constexpr int S = 2048, D = 448, F = 1792;
__device__ __forceinline__ float bf2f(u16 v) { return __uint_as_float(((unsigned)v) << 16); }
__device__ __forceinline__ u16 f2bf(float f) { __hip_bfloat16 b = __float2bfloat16(f); return *(u16*)&b; }

__global__ void k_pre(const u16* x, const u16* k, const u16* v, const u16* sk, float* qn, u16* kv) {
  int s = blockIdx.x; int t = threadIdx.x;
  __shared__ float red[2][8];
  float xv = bf2f(x[s * D + t]);
  float a = xv;
  for (int m = 32; m >= 1; m >>= 1) a += __shfl_xor(a, m);
  if ((t & 63) == 0) red[0][t >> 6] = a;
  __syncthreads();
  float mu = 0; for (int i = 0; i < 7; i++) mu += red[0][i]; mu /= D;
  float dd = xv - mu; float b = dd * dd;
  for (int m = 32; m >= 1; m >>= 1) b += __shfl_xor(b, m);
  if ((t & 63) == 0) red[1][t >> 6] = b;
  __syncthreads();
  float var = 0; for (int i = 0; i < 7; i++) var += red[1][i]; var /= D;
  qn[s * D + t] = dd * rsqrtf(var + 1e-5f);
  float ks = 0;
  for (int j = 0; j < 4; j++) if (s - j >= 0) ks += bf2f(k[(s - j) * D + t]) * bf2f(sk[t * 4 + j]);
  kv[s * D + t] = f2bf(ks * bf2f(v[s * D + t]));
}
__global__ void k_conv(const u16* x, const float* qn, const u16* kv, const u16* ssmk, const u16* ssmD, u16* h, u16* hn) {
  int s = blockIdx.x; int t = threadIdx.x;
  __shared__ float red[2][8];
  float y = 0;
  for (int u = 0; u <= s; u++) y += bf2f(kv[u * D + t]) * bf2f(ssmk[t * S + (s - u)]);
  y += bf2f(kv[s * D + t]) * bf2f(ssmD[t]);
  u16 hb = f2bf(bf2f(x[s * D + t]) + qn[s * D + t] * y);
  h[s * D + t] = hb;
  float xv = bf2f(hb);
  float a = xv;
  for (int m = 32; m >= 1; m >>= 1) a += __shfl_xor(a, m);
  if ((t & 63) == 0) red[0][t >> 6] = a;
  __syncthreads();
  float mu = 0; for (int i = 0; i < 7; i++) mu += red[0][i]; mu /= D;
  float dd = xv - mu; float b = dd * dd;
  for (int m = 32; m >= 1; m >>= 1) b += __shfl_xor(b, m);
  if ((t & 63) == 0) red[1][t >> 6] = b;
  __syncthreads();
  float var = 0; for (int i = 0; i < 7; i++) var += red[1][i]; var /= D;
  hn[s * D + t] = f2bf(dd * rsqrtf(var + 1e-5f));
}
__global__ void k_g1(const u16* hn, const u16* wg, const u16* wu, u16* act) {
  int s = blockIdx.x; int f = blockIdx.y * 256 + threadIdx.x;
  float g = 0, u = 0;
  for (int d = 0; d < D; d++) { float a = bf2f(hn[s * D + d]); g += a * bf2f(wg[d * F + f]); u += a * bf2f(wu[d * F + f]); }
  float ge = 0.5f * g * (1.f + tanhf(0.7978845608028654f * (g + 0.044715f * g * g * g)));
  act[s * F + f] = f2bf(ge * u);
}
__global__ void k_g2(const u16* act, const u16* wd, const u16* h, u16* out) {
  int s = blockIdx.x; int d = threadIdx.x;
  float o = 0;
  for (int f = 0; f < F; f++) o += bf2f(act[s * F + f]) * bf2f(wd[f * D + d]);
  out[s * D + d] = f2bf(bf2f(h[s * D + d]) + o);
}
extern "C" void kernel_launch(void* const* d_in, const int* in_sizes, int n_in,
                              void* d_out, int out_size, void* d_ws, size_t ws_size,
                              hipStream_t stream) {
  const u16* x = (const u16*)d_in[0]; const u16* k = (const u16*)d_in[1]; const u16* v = (const u16*)d_in[2];
  const u16* sk = (const u16*)d_in[3]; const u16* ssmk = (const u16*)d_in[4]; const u16* ssmD = (const u16*)d_in[5];
  const u16* wg = (const u16*)d_in[7]; const u16* wu = (const u16*)d_in[8]; const u16* wd = (const u16*)d_in[9];
  char* ws = (char*)d_ws;
  float* qn = (float*)ws; u16* kv = (u16*)(ws + (4 << 20)); u16* h = (u16*)(ws + (8 << 20)); u16* hn = (u16*)(ws + (12 << 20)); u16* act = (u16*)(ws + (16 << 20));
  k_pre<<<S, D, 0, stream>>>(x, k, v, sk, qn, kv);
  k_conv<<<S, D, 0, stream>>>(x, qn, kv, ssmk, ssmD, h, hn);
  k_g1<<<dim3(S, F / 256), 256, 0, stream>>>(hn, wg, wu, act);
  k_g2<<<S, D, 0, stream>>>(act, wd, h, (u16*)d_out);
}
```

```cpp
#include <hip/hip_runtime.h>
#include <stdint.h>

typedef unsigned short u16;
typedef unsigned u32;
typedef __bf16 bf16x8 __attribute__((ext_vector_type(8)));
typedef __bf16 bf16x2 __attribute__((ext_vector_type(2)));
typedef short s16x4 __attribute__((ext_vector_type(4)));
typedef short s16x8 __attribute__((ext_vector_type(8)));
typedef float f32x2 __attribute__((ext_vector_type(2)));
typedef float f32x4 __attribute__((ext_vector_type(4)));
typedef float f32x16 __attribute__((ext_vector_type(16)));
typedef u32 u32x2 __attribute__((ext_vector_type(2)));
typedef u32 u32x4 __attribute__((ext_vector_type(4)));
#define GAS __attribute__((address_space(1)))
#define LAS __attribute__((address_space(3)))
#define DEV __device__ __forceinline__

constexpr int S = 2048, D = 448, F = 1792;
constexpr int NP = D / 2;
constexpr size_t WS_REC = 0;
constexpr size_t WS_STAT = WS_REC + 256 * NP * 64;
constexpr size_t WS_HT = WS_STAT + 2 * S * 4;
constexpr size_t WS_ACT = WS_HT + (size_t)D * S * 2;

DEV float bflo(u32 v) { return __uint_as_float(v << 16); }
DEV float bfhi(u32 v) { return __uint_as_float(v & 0xffff0000u); }
DEV u32 pk2(float lo, float hi) { f32x2 p = {lo, hi}; return __builtin_bit_cast(u32, __builtin_convertvector(p, bf16x2)); }
DEV float rbf(float x) { return bflo(pk2(x, x)); }
DEV void glds16(const void* g, void* l) { __builtin_amdgcn_global_load_lds((const GAS void*)g, (LAS void*)l, 16, 0, 0); }
DEV s16x4 trd(const char* l) { return __builtin_amdgcn_ds_read_tr16_b64_v4i16((LAS s16x4*)l); }
DEV bf16x8 cat8(s16x4 a, s16x4 b) { return __builtin_bit_cast(bf16x8, __builtin_shufflevector(a, b, 0, 1, 2, 3, 4, 5, 6, 7)); }
DEV float wave_sum(float v) {
#pragma unroll
  for (int m = 32; m >= 1; m >>= 1) v += __shfl_xor(v, m);
  return v;
}
DEV void ln_scalars(float mean, float var, float& m, float& rinv) {
  m = rbf(mean);
  float den = rbf(__builtin_sqrtf(rbf(rbf(var) + 1e-5f)));
  rinv = 1.0f / den;
}

__global__ void __launch_bounds__(512, 2) k_p0(const u32* __restrict__ x, const u32* __restrict__ k, const u32* __restrict__ v,
                                               const u32* __restrict__ shk, char* __restrict__ ws) {
  __shared__ __attribute__((aligned(16))) char smem[16384];
  const int T = blockIdx.x, tid = threadIdx.x, wave = tid >> 6, lane = tid & 63;
  const int s0 = T * 8;
  if (wave < 4) {
    char* wl = smem + wave * 4096;
    if (tid < NP) {
      u32 kk[11], vv[8], xx[8];
#pragma unroll
      for (int i = 0; i < 11; i++) { int r = s0 - 3 + i; kk[i] = k[(r < 0 ? 0 : r) * NP + tid]; if (r < 0) kk[i] = 0; }
#pragma unroll
      for (int i = 0; i < 8; i++) { vv[i] = v[(s0 + i) * NP + tid]; xx[i] = x[(s0 + i) * NP + tid]; }
      u32x4 sk = *(const u32x4*)(shk + tid * 4);
      float w0[4] = {bflo(sk[0]), bfhi(sk[0]), bflo(sk[1]), bfhi(sk[1])};
      float w1[4] = {bflo(sk[2]), bfhi(sk[2]), bflo(sk[3]), bfhi(sk[3])};
      float a0[8], a1[8];
#pragma unroll
      for (int i = 0; i < 8; i++) {
        float s = bflo(kk[i]) * w0[3]; s = fmaf(bflo(kk[i + 1]), w0[2], s); s = fmaf(bflo(kk[i + 2]), w0[1], s); s = fmaf(bflo(kk[i + 3]), w0[0], s);
        a0[i] = s * bflo(vv[i]);
        float t = bfhi(kk[i]) * w1[3]; t = fmaf(bfhi(kk[i + 1]), w1[2], t); t = fmaf(bfhi(kk[i + 2]), w1[1], t); t = fmaf(bfhi(kk[i + 3]), w1[0], t);
        a1[i] = t * bfhi(vv[i]);
      }
      u32x4 r[4];
#pragma unroll
      for (int j = 0; j < 4; j++) {
        r[0][j] = pk2(a0[7 - 2 * j], a0[6 - 2 * j]);
        r[1][j] = pk2(a1[7 - 2 * j], a1[6 - 2 * j]);
        r[2][j] = __builtin_amdgcn_perm(xx[2 * j + 1], xx[2 * j], 0x05040100u);
        r[3][j] = __builtin_amdgcn_perm(xx[2 * j + 1], xx[2 * j], 0x07060302u);
      }
#pragma unroll
      for (int c = 0; c < 4; c++) *(u32x4*)(wl + 64 * lane + 16 * (c ^ ((lane >> 1) & 3))) = r[c];
    }
    const int ni = wave == 3 ? 2 : 4;
    char* dst = ws + WS_REC + (size_t)(T * NP + wave * 64) * 64;
    for (int i = 0; i < ni; i++) {
      int n = 64 * i + lane, r = n >> 2, c = n & 3;
      u32x4 d = *(u32x4*)(wl + 64 * r + 16 * (c ^ ((r >> 1) & 3)));
      *(u32x4*)(dst + n * 16) = d;
    }
  } else {
#pragma unroll
    for (int i = 0; i < 2; i++) {
      const int s = s0 + 2 * (wave - 4) + i;
      u32x4 d = {0, 0, 0, 0};
      if (lane < 56) d = *(const u32x4*)(x + s * NP + lane * 4);
      float f[8];
#pragma unroll
      for (int j = 0; j < 4; j++) { f[2 * j] = bflo(d[j]); f[2 * j + 1] = bfhi(d[j]); }
      float sum = 0;
#pragma unroll
      for (int j = 0; j < 8; j++) sum += f[j];
      float mean = wave_sum(sum) * (1.0f / D);
      float sq = 0;
#pragma unroll
      for (int j = 0; j < 8; j++) { float t = f[j] - mean; sq = fmaf(t, t, sq); }
      if (lane >= 56) sq = 0;
      float var = wave_sum(sq) * (1.0f / D);
      float m, rinv; ln_scalars(mean, var, m, rinv);
      if (lane == 0) { float* st = (float*)(ws + WS_STAT); st[s] = rinv; st[S + s] = -m * rinv; }
    }
  }
}

constexpr int KC_STRIDE = 4160, KC_CH = 4 * KC_STRIDE;
constexpr int L1_KC = 0, L1_REC = 2 * KC_CH  , L1_RED = L1_REC + 288 * 64  , L1_END = L1_RED + 65536;

template <int N0>
DEV void conv_half(const char* kc, const char* rec, int ch, int bq, int lane, f32x4 (&acc)[4]) {
  const int r = lane & 15, kg = lane >> 4;
  const char* abase = kc + ((r + 1) & 3) * KC_STRIDE + 2 * (8 * kg + ((r + 1) & ~3));
#pragma unroll
  for (int bb = 0; bb < 2; bb++) {
    const int b = 2 * bq + bb;
    bf16x8 A[N0 + 4], B[N0 + 4];
#pragma unroll
    for (int a = 0; a < N0 + 4; a++) {
      const char* pa = abase + 64 * b + 512 * a;
      A[a] = cat8(*(const s16x4*)pa, *(const s16x4*)(pa + 8));
      const int g = 254 - 32 * a - 2 * r + 4 * b + kg;
      B[a] = *(const bf16x8*)(rec + 64 * g + 16 * ((ch + (g >> 2)) & 3));
    }
#pragma unroll
    for (int a = 0; a < N0 + 4; a++)
#pragma unroll
      for (int n = N0; n < N0 + 4; n++)
        if (n >= a) acc[n - N0] = __builtin_amdgcn_mfma_f32_16x16x32_bf16(A[a], B[n - a], acc[n - N0], 0, 0, 0);
  }
}

template <int N0>
DEV void conv_finish(char* smem, int ch, int bq, int lane, const f32x4 (&acc)[4], int d, float dcoef, const char* ws_ro, char* ws) {
  char* red = smem + L1_RED + (N0 / 4) * 32768 + ch * 16384;
  const int n = N0 + bq, i = lane & 15, kg = lane >> 4;
  const int s0 = 256 * n + 16 * i + 4 * kg;
  const f32x4 rinv = *(const f32x4*)(ws_ro + WS_STAT + s0 * 4);
  const f32x4 mr = *(const f32x4*)(ws_ro + WS_STAT + (S + s0) * 4);
#pragma unroll
  for (int j = 0; j < 4; j++) *(f32x4*)(red + (bq * 4 + j) * 1024 + lane * 16) = acc[j];
  __syncthreads();
  f32x4 y = *(const f32x4*)(red + (0 * 4 + bq) * 1024 + lane * 16);
#pragma unroll
  for (int j = 1; j < 4; j++) y += *(const f32x4*)(red + (j * 4 + bq) * 1024 + lane * 16);
  const int g = 255 - (s0 >> 3);
  const char* rp = smem + L1_REC + 64 * g;
  const u32x2 kv = *(const u32x2*)(rp + 16 * ((ch + (g >> 2)) & 3) + 8 * (1 - (kg & 1)));
  const u32x2 xv = *(const u32x2*)(rp + 16 * ((2 + ch + (g >> 2)) & 3) + 8 * (kg & 1));
  const float kf[4] = {bfhi(kv[1]), bflo(kv[1]), bfhi(kv[0]), bflo(kv[0])};
  const float xf[4] = {bflo(xv[0]), bfhi(xv[0]), bflo(xv[1]), bfhi(xv[1])};
  float h[4];
#pragma unroll
  for (int j = 0; j < 4; j++) h[j] = fmaf(fmaf(xf[j], rinv[j], mr[j]), fmaf(kf[j], dcoef, y[j]), xf[j]);
  *(u32x2*)(ws + WS_HT + ((size_t)d * S + s0) * 2) = (u32x2){pk2(h[0], h[1]), pk2(h[2], h[3])};
}

__global__ void __launch_bounds__(512, 2) k_p1(const u16* __restrict__ ssmk, const u16* __restrict__ ssmd, char* __restrict__ ws) {
  extern __shared__ __attribute__((aligned(16))) char smem[];
  const int bx = blockIdx.x, p = (bx & 7) * 28 + (bx >> 3);
  const int tid = threadIdx.x, wave = __builtin_amdgcn_readfirstlane(tid >> 6), lane = tid & 63;
  const int ch = wave >> 2, bq = wave & 3;
#pragma unroll
  for (int i = 0; i < 2; i++) {
    const int n = (wave * 2 + i) * 64 + lane, g = n >> 2, c = ((n & 3) - (g >> 2)) & 3;
    glds16(ws + WS_REC + (size_t)((255 - g) * NP + p) * 64 + c * 16, smem + L1_REC + (wave * 2 + i) * 1024);
  }
  {
    const u16* kr = ssmk + (size_t)(2 * p + ch) * S;
    char* kc = smem + L1_KC + ch * KC_CH + bq * KC_STRIDE;
#pragma unroll
    for (int i = 0; i < 4; i++) {
      const int q = 2 + 64 * i + lane;
      if (q <= 256) glds16(kr + 8 * q + bq - 16, kc + (2 + 64 * i) * 16);
    }
    if (lane < 3) {
      u32x4 t = *(const u32x4*)(kr + (lane == 2 ? S - 8 : 0));
      unsigned __int128 w = ((unsigned __int128)(((uint64_t)t[3] << 32) | t[2]) << 64) | (((uint64_t)t[1] << 32) | t[0]);
      if (lane == 0 || (lane == 1 && bq == 0)) w = 0;
      else if (lane == 1) w <<= 16 * (8 - bq);
      else w >>= 16 * bq;
      u32x4 o = {(u32)w, (u32)(w >> 32), (u32)(w >> 64), (u32)(w >> 96)};
      *(u32x4*)(kc + (lane == 2 ? 257 : lane) * 16) = o;
    }
  }
  if (tid < 128) *(u32x4*)(smem + L1_REC + 256 * 64 + tid * 16) = (u32x4){0, 0, 0, 0};
  const float dcoef = __uint_as_float((u32)ssmd[2 * p + ch] << 16);
  __syncthreads();
  const char* kc = smem + L1_KC + ch * KC_CH;
  f32x4 acc[4] = {};
  conv_half<0>(kc, smem + L1_REC, ch, bq, lane, acc);
  conv_finish<0>(smem, ch, bq, lane, acc, 2 * p + ch, dcoef, ws, ws);
  f32x4 acc2[4] = {};
  conv_half<4>(kc, smem + L1_REC, ch, bq, lane, acc2);
  conv_finish<4>(smem, ch, bq, lane, acc2, 2 * p + ch, dcoef, ws, ws);
}

constexpr int L2_W = 0;
constexpr int L2_PART = 114688;
constexpr int L2_END = L2_PART + 4096;

DEV float gelu_mul(float g, float u) {
  const float c = -2.0f * 0.7978845608028654f * 1.4426950408889634f;
  float z = g * fmaf(g * g, 0.044715f * c, c);
  return g * u * __builtin_amdgcn_rcpf(1.0f + __builtin_amdgcn_exp2f(z));
}

__global__ void __launch_bounds__(512, 2) k_g1(const u16* __restrict__ wg, const u16* __restrict__ wu, char* __restrict__ ws) {
  extern __shared__ __attribute__((aligned(16))) char smem[];
  const int bx = blockIdx.x, xc = bx & 7, li = bx >> 3;
  const int tg = (xc & 3) * 4 + (li & 3), fs = (xc >> 2) * 7 + (li >> 2);
  const int tid = threadIdx.x, wave = __builtin_amdgcn_readfirstlane(tid >> 6), lane = tid & 63;
  const int grp = lane >> 4, hi = grp >> 1, q = (lane & 15) >> 2, pp = lane & 3;
  bf16x8 W[28];
  auto stage_w = [&](int bt) {
    char* dst = smem + L2_W + (bt & 1) * 57344;
#pragma unroll
    for (int i = 0; i < 7; i++) {
      const int u = wave * 7 + i, blk = u >= 28, ri = u - 28 * blk;
      const int row = 4 * ri + grp, c = (lane & 15) ^ (2 * grp + 8 * blk);
      glds16((blk ? wu : wg) + (size_t)(bt * 112 + row) * F + fs * 128 + 8 * c, dst + u * 1024);
    }
  };
  stage_w(0);
#pragma unroll
  for (int bt = 0; bt < 4; bt++) {
    __syncthreads();
    if (bt < 3) stage_w(bt + 1);
    const int blk = grp & 1;
    const char* src = smem + L2_W + (bt & 1) * 57344 + blk * 28672 + (8 * hi + q) * 256 + 16 * ((2 * wave + (pp >> 1)) ^ (2 * q + 8 * blk)) + 8 * (pp & 1);
#pragma unroll
    for (int ks = 0; ks < 7; ks++) W[bt * 7 + ks] = cat8(trd(src + ks * 4096), trd(src + ks * 4096 + 1024));
  }
  __syncthreads();
  const char* hT = ws + WS_HT;
#pragma unroll
  for (int qq = 0; qq < 4; qq++)
#pragma unroll
    for (int i = 0; i < 4; i++) {
      const int ks = wave + 8 * i;
      if (ks < 28) glds16(hT + ((size_t)(16 * ks + (lane >> 2)) * S + 32 * (tg + 16 * qq)) * 2 + 16 * (lane & 3), smem + qq * 28672 + ks * 1024);
    }
  f32x16 cs = {};
  {
    const bf16x8 ones = __builtin_bit_cast(bf16x8, (u32x4){0x3f803f80u, 0x3f803f80u, 0x3f803f80u, 0x3f803f80u});
#pragma unroll
    for (int ks = 0; ks < 28; ks++) cs = __builtin_amdgcn_mfma_f32_32x32x16_bf16(W[ks], ones, cs, 0, 0, 0);
  }
  __syncthreads();
  const int boff = (8 * hi + q) * 64 + (grp & 1) * 32 + pp * 8;
#pragma unroll
  for (int qq = 0; qq < 4; qq++) {
    const char* hb = smem + qq * 28672 + boff;
    f32x16 acc = {};
    float s1 = 0, s2 = 0;
#pragma unroll
    for (int ks = 0; ks < 28; ks++) {
      bf16x8 b = cat8(trd(hb + ks * 1024), trd(hb + ks * 1024 + 256));
      acc = __builtin_amdgcn_mfma_f32_32x32x16_bf16(W[ks], b, acc, 0, 0, 0);
      if ((ks & 7) == wave) {
        const bf16x2 one2 = __builtin_bit_cast(bf16x2, 0x3f803f80u);
        bf16x2 p0 = __builtin_shufflevector(b, b, 0, 1), p1 = __builtin_shufflevector(b, b, 2, 3), p2 = __builtin_shufflevector(b, b, 4, 5), p3 = __builtin_shufflevector(b, b, 6, 7);
        s1 = __builtin_amdgcn_fdot2_f32_bf16(p0, one2, s1, false); s2 = __builtin_amdgcn_fdot2_f32_bf16(p0, p0, s2, false);
        s1 = __builtin_amdgcn_fdot2_f32_bf16(p1, one2, s1, false); s2 = __builtin_amdgcn_fdot2_f32_bf16(p1, p1, s2, false);
        s1 = __builtin_amdgcn_fdot2_f32_bf16(p2, one2, s1, false); s2 = __builtin_amdgcn_fdot2_f32_bf16(p2, p2, s2, false);
        s1 = __builtin_amdgcn_fdot2_f32_bf16(p3, one2, s1, false); s2 = __builtin_amdgcn_fdot2_f32_bf16(p3, p3, s2, false);
      }
    }
    s1 += __shfl_xor(s1, 32); s2 += __shfl_xor(s2, 32);
    char* part = smem + L2_PART + (qq & 1) * 2048;
    if (lane < 32) *(f32x2*)(part + (wave >> 1) * 512 + lane * 16 + (wave & 1) * 8) = (f32x2){s1, s2};
    __syncthreads();
    float t1 = 0, t2 = 0;
#pragma unroll
    for (int j = 0; j < 4; j++) { f32x4 t = *(const f32x4*)(part + j * 512 + (lane & 31) * 16); t1 += t[0] + t[2]; t2 += t[1] + t[3]; }
    const float mean = t1 * (1.0f / D);
    float m, rinv; ln_scalars(mean, fmaxf(fmaf(-mean, mean, t2 * (1.0f / D)), 0.0f), m, rinv);
    u32x4 o;
#pragma unroll
    for (int j = 0; j < 4; j++) {
      float a[2];
#pragma unroll
      for (int e = 0; e < 2; e++) {
        const int r = 2 * j + e;
        a[e] = gelu_mul(fmaf(cs[r], -m, acc[r]) * rinv, fmaf(cs[r + 8], -m, acc[r + 8]) * rinv);
      }
      o[j] = pk2(a[0], a[1]);
    }
    *(u32x4*)(ws + WS_ACT + ((size_t)((tg + 16 * qq) * 112 + fs * 8 + wave) * 64 + lane) * 16) = o;
  }
}

constexpr int L3_W = 0;
constexpr int L3_H = 114688;
constexpr int L3_END = L3_H + 8192;

__global__ void __launch_bounds__(512, 2) k_g2(const u16* __restrict__ wd, const char* __restrict__ ws, u16* __restrict__ out) {
  extern __shared__ __attribute__((aligned(16))) char smem[];
  const int bx = blockIdx.x, xc = bx & 7, li = bx >> 3;
  const int tg = xc * 4 + (li & 3), ds = li >> 2;
  const int tid = threadIdx.x, wave = __builtin_amdgcn_readfirstlane(tid >> 6), lane = tid & 63;
  const int grp = lane >> 4, hi = grp >> 1, q = (lane & 15) >> 2, pp = lane & 3;
  bf16x8 W[14][2];
  char* wl = smem + L3_W + wave * 14336;
  if (wave < 4)
#pragma unroll
    for (int hf = 0; hf < 2; hf++)
      glds16(ws + WS_HT + ((size_t)(64 * ds + 16 * wave + (lane >> 2)) * S + 32 * (tg + 32 * hf)) * 2 + 16 * (lane & 3), smem + L3_H + hf * 4096 + wave * 1024);
#pragma unroll
  for (int rd = 0; rd < 2; rd++) {
#pragma unroll
    for (int i = 0; i < 14; i++) {
      const int row = 8 * i + (lane >> 3), c = (lane & 7) ^ (4 * ((row >> 1) & 1));
      glds16(wd + (size_t)(224 * wave + 112 * rd + row) * D + 64 * ds + 8 * c, wl + i * 1024);
    }
    asm volatile("s_waitcnt vmcnt(0)" ::: "memory");
    const char* src = wl + (4 * hi + q) * 128 + (grp & 1) * 32 + pp * 8;
#pragma unroll
    for (int ks = 0; ks < 7; ks++)
#pragma unroll
      for (int dt = 0; dt < 2; dt++) {
        const char* a = src + ks * 2048 + 64 * (dt ^ (q >> 1));
        W[rd * 7 + ks][dt] = cat8(trd(a), trd(a + 1024));
      }
    asm volatile("s_waitcnt lgkmcnt(0)" ::: "memory");
  }
  __syncthreads();
#pragma unroll
  for (int hf = 0; hf < 2; hf++) {
    const int tt = tg + 32 * hf;
    const bf16x8* ap = (const bf16x8*)(ws + WS_ACT) + (size_t)(tt * 112 + 14 * wave) * 64 + lane;
    bf16x8 B[14];
#pragma unroll
    for (int ks = 0; ks < 14; ks++) B[ks] = ap[ks * 64];
    f32x16 acc[2] = {};
    if (wave < 4) {
      const char* hb = smem + L3_H + hf * 4096 + wave * 1024 + (8 * hi + q) * 64 + (grp & 1) * 32 + pp * 8;
      bf16x8 hfr = cat8(trd(hb), trd(hb + 256));
      const int j = (lane & 31) - 16 * (wave & 1) - 8 * hi;
      u32x4 id;
#pragma unroll
      for (int e = 0; e < 4; e++) id[e] = (j == 2 * e ? 0x3f80u : 0u) | (j == 2 * e + 1 ? 0x3f800000u : 0u);
      f32x16 z = {};
      f32x16 rres = __builtin_amdgcn_mfma_f32_32x32x16_bf16(__builtin_bit_cast(bf16x8, id), hfr, z, 0, 0, 0);
      if (wave < 2) acc[0] = rres; else acc[1] = rres;
    }
#pragma unroll
    for (int ks = 0; ks < 14; ks++) {
      acc[0] = __builtin_amdgcn_mfma_f32_32x32x16_bf16(W[ks][0], B[ks], acc[0], 0, 0, 0);
      acc[1] = __builtin_amdgcn_mfma_f32_32x32x16_bf16(W[ks][1], B[ks], acc[1], 0, 0, 0);
    }
    const int tok = lane & 31;
    char* red = smem + L3_W + wave * 8192 + tok * 256;
#pragma unroll
    for (int dt = 0; dt < 2; dt++)
#pragma unroll
      for (int g = 0; g < 4; g++)
        *(f32x4*)(red + 16 * ((8 * dt + 2 * g + hi) ^ (tok & 15))) = (f32x4){acc[dt][4 * g], acc[dt][4 * g + 1], acc[dt][4 * g + 2], acc[dt][4 * g + 3]};
    __syncthreads();
    {
      const int tk = tid >> 4, sl = tid & 15;
      const char* rp = smem + L3_W + tk * 256 + 16 * (sl ^ (tk & 15));
      f32x4 o = *(const f32x4*)rp;
#pragma unroll
      for (int w = 1; w < 8; w++) o += *(const f32x4*)(rp + w * 8192);
      *(u32x2*)(out + (size_t)(32 * tt + tk) * D + 64 * ds + 4 * sl) = (u32x2){pk2(o[0], o[1]), pk2(o[2], o[3])};
    }
    __syncthreads();
  }
}

extern "C" void kernel_launch(void* const* d_in, const int* in_sizes, int n_in, void* d_out, int out_size, void* d_ws, size_t ws_size,
                              hipStream_t stream) {
  char* ws = (char*)d_ws;
  (void)hipFuncSetAttribute((const void*)k_p1, hipFuncAttributeMaxDynamicSharedMemorySize, L1_END);
  (void)hipFuncSetAttribute((const void*)k_g1, hipFuncAttributeMaxDynamicSharedMemorySize, L2_END);
  (void)hipFuncSetAttribute((const void*)k_g2, hipFuncAttributeMaxDynamicSharedMemorySize, L3_END);
  k_p0<<<256, 512, 0, stream>>>((const u32*)d_in[0], (const u32*)d_in[1], (const u32*)d_in[2], (const u32*)d_in[3], ws);
  k_p1<<<224, 512, L1_END, stream>>>((const u16*)d_in[4], (const u16*)d_in[5], ws);
  k_g1<<<224, 512, L2_END, stream>>>((const u16*)d_in[7], (const u16*)d_in[8], ws);
  k_g2<<<224, 512, L3_END, stream>>>((const u16*)d_in[9], ws, (u16*)d_out);
}
```

```cpp
#include <hip/hip_runtime.h>
#include <stdint.h>

typedef unsigned short u16;
typedef unsigned u32;
typedef unsigned long long u64;
typedef __bf16 bf16x8 __attribute__((ext_vector_type(8)));
typedef __bf16 bf16x2 __attribute__((ext_vector_type(2)));
typedef short s16x4 __attribute__((ext_vector_type(4)));
typedef float f32x2 __attribute__((ext_vector_type(2)));
typedef float f32x4 __attribute__((ext_vector_type(4)));
typedef float f32x16 __attribute__((ext_vector_type(16)));
typedef u32 u32x2 __attribute__((ext_vector_type(2)));
typedef u32 u32x4 __attribute__((ext_vector_type(4)));
#define LAS __attribute__((address_space(3)))
#define DEV __device__ __forceinline__
typedef LAS char* lptr;

constexpr int S = 2048, D = 448, F = 1792, NP = D / 2;
constexpr u32 WS_REC = 0;
constexpr u32 WS_STAT = WS_REC + 256 * NP * 64;
constexpr u32 WS_HT = WS_STAT + S * 8;
constexpr u32 WS_ACT = WS_HT + D * S * 2;
constexpr u32 WS_F0 = WS_ACT + S * F * 2;
constexpr u32 WS_FC = WS_F0 + 8 * 4 * 256 * 16;
constexpr u32 WS_FG = WS_FC + 32 * 256 * 16;
constexpr u32 WS_NONCE = WS_FG + 64 * 128 * 16;
constexpr u32 WS_DBG = WS_NONCE + 4096;
constexpr int L_RED = 65536, L_KC = 98304, KC_ST = 4128, L_REC = L_KC + 8 * KC_ST;
constexpr int L_H = 0, H_BYTES = 28672, L_PART = 2 * H_BYTES, L_WD = 65536, WD_ST = 12288, L_RED2 = 32768;
constexpr int SPIN_CAP = 1 << 17;

#define STAMP(i)
#define STAMP_DECL
#define STAMP_OUT()

DEV float bflo(u32 v) { return __uint_as_float(v << 16); }
DEV float bfhi(u32 v) { return __uint_as_float(v & 0xffff0000u); }
DEV u32 pk2(float lo, float hi) { f32x2 p = {lo, hi}; return __builtin_bit_cast(u32, __builtin_convertvector(p, bf16x2)); }
DEV float rbf(float x) { return bflo(pk2(x, x)); }
DEV s16x4 trd(lptr l) { return __builtin_amdgcn_ds_read_tr16_b64_v4i16((LAS s16x4*)l); }
DEV bf16x8 cat8(s16x4 a, s16x4 b) { return __builtin_bit_cast(bf16x8, __builtin_shufflevector(a, b, 0, 1, 2, 3, 4, 5, 6, 7)); }
template <int CTRL> DEV float dpp_add(float v) { return v + __int_as_float(__builtin_amdgcn_update_dpp(0, __float_as_int(v), CTRL, 0xf, 0xf, true)); }
DEV float sum32(float v) { auto r = __builtin_amdgcn_permlane32_swap(__float_as_uint(v), __float_as_uint(v), false, false); return __uint_as_float(r[0]) + __uint_as_float(r[1]); }
DEV float wave_sum(float v) {
  v = dpp_add<0xB1>(v); v = dpp_add<0x4E>(v); v = dpp_add<0x141>(v); v = dpp_add<0x140>(v);
  auto r = __builtin_amdgcn_permlane16_swap(__float_as_uint(v), __float_as_uint(v), false, false);
  return sum32(__uint_as_float(r[0]) + __uint_as_float(r[1]));
}
DEV float ln_den(float var) { return rbf(__builtin_sqrtf(rbf(rbf(var) + 1e-5f))); }

DEV void glds(const void* base, u32 voff, u32 lds) {
  asm volatile("s_mov_b32 m0, %2\n\ts_nop 0\n\tglobal_load_lds_dwordx4 %0, %1" :: "v"(voff), "s"(base), "s"(lds) : "memory");
}
DEV void glds_sc1(const void* base, u32 voff, u32 lds) {
  asm volatile("s_mov_b32 m0, %2\n\ts_nop 0\n\tglobal_load_lds_dwordx4 %0, %1 sc1" :: "v"(voff), "s"(base), "s"(lds) : "memory");
}
DEV void st16_sc1(const void* base, u32 voff, u32x4 d) { asm volatile("global_store_dwordx4 %0, %1, %2 sc1\n\ts_nop 1" :: "v"(voff), "v"(d), "s"(base) : "memory"); }
DEV void st8_sc1(const void* base, u32 voff, u32x2 d) { asm volatile("global_store_dwordx2 %0, %1, %2 sc1" :: "v"(voff), "v"(d), "s"(base) : "memory"); }
#define WAIT_VM(n) asm volatile("s_waitcnt vmcnt(" #n ")" ::: "memory")
#define BARRIER() asm volatile("s_waitcnt lgkmcnt(0)\n\ts_barrier" ::: "memory")
DEV void poll(const void* base, u32 voff, u32x2 tag) {
#pragma unroll 1
  for (int it = 0; it < SPIN_CAP; it++) {
    u32x2 f;
    asm volatile("global_load_dwordx2 %0, %1, %2 sc1\n\ts_waitcnt vmcnt(0)" : "=&v"(f) : "v"(voff), "s"(base) : "memory");
    if (__all(f[0] == tag[0] && f[1] == tag[1])) break;
    __builtin_amdgcn_s_sleep(1);
  }
}
DEV u32x2 make_tag(u32x2 raw) {
  const u64 n = (((u64)(u32)__builtin_amdgcn_readfirstlane(raw[1]) << 32) | (u32)__builtin_amdgcn_readfirstlane(raw[0])) + 0x9E3779B97F4A7C15ull;
  return (u32x2){(u32)n, (u32)(n >> 32)};
}

template <int K0, int NKS> DEV void stage_w1(const u16* wg, const u16* wu, int fs, int w4, int lane) {
  const int grp = lane >> 4;
#pragma unroll
  for (int blk = 0; blk < 2; blk++) {
    const u32 voff = grp * (F * 2) + 16 * ((lane & 15) ^ (2 * grp + 8 * blk));
    const char* base = (const char*)(blk ? wu : wg) + (size_t)(16 * K0 + 4 * w4 * NKS) * (F * 2) + fs * 256;
#pragma unroll
    for (int i = 0; i < NKS; i++) glds(base + i * 4 * (F * 2), voff, blk * NKS * 4096 + (w4 * NKS + i) * 1024);
  }
}
template <int K0, int NKS> DEV void read_w1(lptr sm, bf16x8 (&W)[28], f32x16& cs, int wave, int lane, bool docs) {
  const int grp = lane >> 4, hi = grp >> 1, blk = grp & 1, q = (lane & 15) >> 2, pp = lane & 3;
  lptr src = sm + blk * NKS * 4096 + (8 * hi + q) * 256 + 16 * ((2 * wave + (pp >> 1)) ^ (2 * q + 8 * blk)) + 8 * (pp & 1);
#pragma unroll
  for (int ks = 0; ks < NKS; ks++) W[K0 + ks] = cat8(trd(src + ks * 4096), trd(src + ks * 4096 + 1024));
}
template <int K0, int NKS> DEV void colsum(const bf16x8 (&W)[28], f32x16& cs) {
  const bf16x8 ones = __builtin_bit_cast(bf16x8, (u32x4){0x3f803f80u, 0x3f803f80u, 0x3f803f80u, 0x3f803f80u});
#pragma unroll
  for (int ks = 0; ks < NKS; ks++) cs = __builtin_amdgcn_mfma_f32_32x32x16_bf16(W[K0 + ks], ones, cs, 0, 0, 0);
}

template <int C> DEV void conv_mma(lptr sm, int ch, int bq, int lane) {
  constexpr int NF = 2 * C + 2;
  const int r = lane & 15, kg = lane >> 4;
  lptr abase = sm + L_KC + (2 * ((r + 1) & 3) + ch) * KC_ST + 2 * (8 * kg + ((r + 1) & ~3)) + 128 * bq;
  f32x4 acc[2] = {};
#pragma unroll
  for (int bb = 0; bb < 2; bb++) {
    bf16x8 A[NF], B[NF];
    const int g0 = 254 - 2 * r + 4 * (2 * bq + bb) + kg;
    lptr bbase = sm + L_REC + 64 * g0 + 16 * ((ch + (g0 >> 2)) & 3);
#pragma unroll
    for (int a = 0; a < NF; a++) {
      lptr pa = abase + 64 * bb + 512 * a;
      A[a] = cat8(*(LAS s16x4*)pa, *(LAS s16x4*)(pa + 8));
      B[a] = *(LAS bf16x8*)(bbase - 2048 * a);
    }
#pragma unroll
    for (int a = 0; a < NF; a++) {
      if (a <= 2 * C) acc[0] = __builtin_amdgcn_mfma_f32_16x16x32_bf16(A[a], B[2 * C - a], acc[0], 0, 0, 0);
      acc[1] = __builtin_amdgcn_mfma_f32_16x16x32_bf16(A[a], B[2 * C + 1 - a], acc[1], 0, 0, 0);
    }
  }
  lptr red = sm + L_RED + (C & 1) * 16384 + (ch * 4 + bq) * 2048 + kg * 256 + ((r + 4 * kg) & 15) * 16;
  *(LAS f32x4*)red = acc[0];
  *(LAS f32x4*)(red + 1024) = acc[1];
}
template <int C> DEV void conv_fin(lptr sm, const char* ws, int p, int j, int lane, float d0, float d1, u32 tag) {
  const int kg = lane & 3, ib = (lane >> 2) & 1, ch = (lane >> 3) & 1, ttl = lane >> 4;
  const int nl = j >> 1, i = 8 * (j & 1) + 2 * ttl + ib;
  const int s0 = 256 * (2 * C + nl) + 16 * i + 4 * kg;
  u32x4 sa, sb;
#pragma unroll 1
  for (int it = 0; it < SPIN_CAP; it++) {
    asm volatile("global_load_dwordx4 %0, %2, %3 sc1\n\tglobal_load_dwordx4 %1, %2, %3 offset:16 sc1\n\ts_waitcnt vmcnt(0)"
                 : "=&v"(sa), "=&v"(sb) : "v"(s0 * 8), "s"(ws + WS_STAT) : "memory");
    if (__all(sa[1] == tag && sa[3] == tag && sb[1] == tag && sb[3] == tag)) break;
  }
  lptr red = sm + L_RED + (C & 1) * 16384 + ch * 8192 + nl * 1024 + kg * 256 + ((i + 4 * kg) & 15) * 16;
  f32x4 y = *(LAS f32x4*)red;
#pragma unroll
  for (int b = 1; b < 4; b++) y += *(LAS f32x4*)(red + b * 2048);
  const int g = 255 - (s0 >> 3);
  lptr rp = sm + L_REC + 64 * g;
  const u32x2 kv = *(LAS u32x2*)(rp + 16 * ((ch + (g >> 2)) & 3) + 8 * (1 - (kg & 1)));
  const u32x2 xv = *(LAS u32x2*)(rp + 16 * ((2 + ch + (g >> 2)) & 3) + 8 * (kg & 1));
  const float kf[4] = {bfhi(kv[1]), bflo(kv[1]), bfhi(kv[0]), bflo(kv[0])};
  const float xf[4] = {bflo(xv[0]), bfhi(xv[0]), bflo(xv[1]), bfhi(xv[1])};
  const u32 st[4] = {sa[0], sa[2], sb[0], sb[2]};
  const float dc = ch ? d1 : d0;
  float h[4];
#pragma unroll
  for (int e = 0; e < 4; e++) h[e] = fmaf((xf[e] - bflo(st[e])) * __builtin_amdgcn_rcpf(bfhi(st[e])), fmaf(kf[e], dc, y[e]), xf[e]);
  st8_sc1(ws + WS_HT, (s0 >> 5) * H_BYTES + (2 * p + ch) * 64 + (s0 & 31) * 2, (u32x2){pk2(h[0], h[1]), pk2(h[2], h[3])});
}
template <int C> DEV void conv_pub(const char* ws, int p, int j, int lane, u32x2 tag) {
  WAIT_VM(0);
  if (lane < 2) st8_sc1(ws + WS_FC, (((C * 4 + j) * 2 + lane) * 256 + p) * 16, tag);
}

DEV float gelu_mul(float g, float u) {
  const float c = -2.0f * 0.7978845608028654f * 1.4426950408889634f;
  const float z = g * fmaf(g * g, 0.044715f * c, c);
  return g * u * __builtin_amdgcn_rcpf(1.0f + __builtin_amdgcn_exp2f(z));
}
template <int Q> DEV void load_h(const char* ws, int tg, int rep, int wave, int lane, u32x2 tag) {
  poll(ws + WS_FC, (((Q * 4 + (tg >> 2)) * 2 + rep) * 256 + 56 * wave + (lane < 56 ? lane : 0)) * 16, tag);
  const char* base = ws + WS_HT + (16 * Q + tg) * H_BYTES + wave * 7168;
#pragma unroll
  for (int i = 0; i < 7; i++) glds_sc1(base + i * 1024, lane * 16, L_H + (Q & 1) * H_BYTES + wave * 7168 + i * 1024);
}
template <int Q> DEV void g1_stats(lptr sm, int wave, int lane) {
  const int grp = lane >> 4, q = (lane & 15) >> 2, pp = lane & 3;
  lptr hb = sm + L_H + (Q & 1) * H_BYTES + (8 * (grp >> 1) + q) * 64 + (grp & 1) * 32 + pp * 8 + wave * 1024;
  float s1 = 0, s2 = 0;
  const bf16x2 one2 = __builtin_bit_cast(bf16x2, 0x3f803f80u);
#pragma unroll
  for (int i = 0; i < 4; i++) {
    lptr a = hb + (i < 3 ? i * 8192 : (wave < 4 ? 24576 : 0));
    s16x4 u = trd(a), v = trd(a + 256);
    u32x2 uu = __builtin_bit_cast(u32x2, u), vv = __builtin_bit_cast(u32x2, v);
    u32 w[4] = {uu[0], uu[1], vv[0], vv[1]};
#pragma unroll
    for (int e = 0; e < 4; e++) {
      if (i == 3) w[e] = wave < 4 ? w[e] : 0u;
      bf16x2 t = __builtin_bit_cast(bf16x2, w[e]);
      s1 = __builtin_amdgcn_fdot2_f32_bf16(t, one2, s1, false);
      s2 = __builtin_amdgcn_fdot2_f32_bf16(t, t, s2, false);
    }
  }
  s1 = sum32(s1); s2 = sum32(s2);
  if (lane < 32) *(LAS f32x2*)(sm + L_PART + (Q & 1) * 2048 + (wave >> 1) * 512 + lane * 16 + (wave & 1) * 8) = (f32x2){s1, s2};
}
template <int Q> DEV void g1_chain(lptr sm, const bf16x8 (&W)[28], f32x16& acc, int lane) {
  const int grp = lane >> 4, q = (lane & 15) >> 2, pp = lane & 3;
  lptr hb = sm + L_H + (Q & 1) * H_BYTES + (8 * (grp >> 1) + q) * 64 + (grp & 1) * 32 + pp * 8;
  acc = (f32x16){};
#pragma unroll
  for (int ks = 0; ks < 28; ks++) acc = __builtin_amdgcn_mfma_f32_32x32x16_bf16(W[ks], cat8(trd(hb + ks * 1024), trd(hb + ks * 1024 + 256)), acc, 0, 0, 0);
}
template <int Q> DEV u32x4 g1_epi(lptr sm, const f32x16& acc, const f32x16& cs, int lane) {
  float t1 = 0, t2 = 0;
#pragma unroll
  for (int j = 0; j < 4; j++) { f32x4 t = *(LAS f32x4*)(sm + L_PART + (Q & 1) * 2048 + j * 512 + (lane & 31) * 16); t1 += t[0] + t[2]; t2 += t[1] + t[3]; }
  const float mean = t1 * (1.0f / D);
  const float m = rbf(mean), rinv = __builtin_amdgcn_rcpf(ln_den(fmaxf(fmaf(-mean, mean, t2 * (1.0f / D)), 0.0f)));
  u32x4 o;
#pragma unroll
  for (int j = 0; j < 4; j++)
    o[j] = pk2(gelu_mul(fmaf(cs[2 * j], -m, acc[2 * j]) * rinv, fmaf(cs[2 * j + 8], -m, acc[2 * j + 8]) * rinv),
               gelu_mul(fmaf(cs[2 * j + 1], -m, acc[2 * j + 1]) * rinv, fmaf(cs[2 * j + 9], -m, acc[2 * j + 9]) * rinv));
  return o;
}
template <int Q> DEV void g1_store(const char* ws, int tg, int ft, int lane, u32x4 o) {
  st16_sc1(ws + WS_ACT, (((16 * Q + tg) * 2 + ((lane >> 4) & 1)) * 112 + ft) * 512 + (lane >> 5) * 256 + (lane & 15) * 16, o);
}
template <int Q> DEV void g1_flag(const char* ws, int tg, int ft, int lane, u32x2 tag) {
  if (lane == 0) st8_sc1(ws + WS_FG, ((16 * Q + tg) * 128 + ft) * 16, tag);
}
DEV void stage_wd(const u16* wd, int ds, int wv, int ks, int lane, u32 lds) {
  const u32 voff = (lane >> 3) * (D * 2) + 16 * ((lane & 7) ^ (2 * ((lane >> 4) & 3)));
  const char* base = (const char*)wd + (size_t)(224 * wv + 32 * ks) * (D * 2) + ds * 128;
#pragma unroll
  for (int i = 0; i < 4; i++) glds(base + i * 8 * (D * 2), voff, lds + i * 1024);
}
DEV void read_wd(lptr src, bf16x8 (&Wk)[4], int lane) {
  const int kg = lane >> 4, q = (lane & 15) >> 2, pp = lane & 3;
  const int row = 16 * (kg >> 1) + 4 * (kg & 1) + q;
#pragma unroll
  for (int dt = 0; dt < 4; dt++) {
    lptr a = src + row * 128 + 32 * (dt ^ ((row >> 1) & 3)) + 8 * pp;
    Wk[dt] = cat8(trd(a), trd(a + 1024));
  }
}

__global__ void __launch_bounds__(512, 2)
k_fused(const u32* __restrict__ x, const u32* __restrict__ k, const u32* __restrict__ v, const u32* __restrict__ shk,
        const u16* __restrict__ ssmk, const u16* __restrict__ ssmd, const u16* __restrict__ wg, const u16* __restrict__ wu,
        const u16* __restrict__ wd, char* __restrict__ ws, u16* __restrict__ out) {
  __shared__ __attribute__((aligned(256))) char smem[163840];
  lptr sm = (lptr)smem;
  const int bx = blockIdx.x, xc = bx & 7, li = bx >> 3;
  const int tid = threadIdx.x, wave = __builtin_amdgcn_readfirstlane(tid >> 6), lane = tid & 63;
  const bool worker = li < 28;
  const int p = xc * 28 + li;
  const int tg = (xc & 3) * 4 + (li & 3), fs = (xc >> 2) * 7 + (li >> 2);
  const int T = xc * 32 + li, s0 = T * 8;
  u32x2 tag;
  STAMP_DECL;
  STAMP(0);
  if (wave < 4) {
    lptr wl = sm + L_REC + wave * 4096;
    const u64 raw = __hip_atomic_load((const __attribute__((address_space(1))) u64*)(ws + WS_NONCE), __ATOMIC_RELAXED, __HIP_MEMORY_SCOPE_AGENT);
    if (tid < NP) {
      u32 kk[11], vv[8], xx[8];
#pragma unroll
      for (int i = 0; i < 11; i++) { const int r = s0 - 3 + i; kk[i] = k[(r < 0 ? 0 : r) * NP + tid]; if (r < 0) kk[i] = 0; }
#pragma unroll
      for (int i = 0; i < 8; i++) { vv[i] = v[(s0 + i) * NP + tid]; xx[i] = x[(s0 + i) * NP + tid]; }
      const u32x4 sk = *(const u32x4*)(shk + tid * 4);
      const float w0[4] = {bflo(sk[0]), bfhi(sk[0]), bflo(sk[1]), bfhi(sk[1])};
      const float w1[4] = {bflo(sk[2]), bfhi(sk[2]), bflo(sk[3]), bfhi(sk[3])};
      float a0[8], a1[8];
#pragma unroll
      for (int i = 0; i < 8; i++) {
        float s = bflo(kk[i]) * w0[3]; s = fmaf(bflo(kk[i + 1]), w0[2], s); s = fmaf(bflo(kk[i + 2]), w0[1], s); s = fmaf(bflo(kk[i + 3]), w0[0], s);
        a0[i] = s * bflo(vv[i]);
        float t = bfhi(kk[i]) * w1[3]; t = fmaf(bfhi(kk[i + 1]), w1[2], t); t = fmaf(bfhi(kk[i + 2]), w1[1], t); t = fmaf(bfhi(kk[i + 3]), w1[0], t);
        a1[i] = t * bfhi(vv[i]);
      }
      u32x4 r[4];
#pragma unroll
      for (int j = 0; j < 4; j++) {
        r[0][j] = pk2(a0[7 - 2 * j], a0[6 - 2 * j]);
        r[1][j] = pk2(a1[7 - 2 * j], a1[6 - 2 * j]);
        r[2][j] = __builtin_amdgcn_perm(xx[2 * j + 1], xx[2 * j], 0x05040100u);
        r[3][j] = __builtin_amdgcn_perm(xx[2 * j + 1], xx[2 * j], 0x07060302u);
      }
#pragma unroll
      for (int c = 0; c < 4; c++) *(LAS u32x4*)(wl + 64 * lane + 16 * (c ^ ((lane >> 1) & 3))) = r[c];
    } else if (worker && tid < NP + 24) {
      const int u = tid - NP, ci = u & 7, which = u >> 3, sg = ci >> 1;
      const u32x4 t = *(const u32x4*)(ssmk + (size_t)(2 * p + (ci & 1)) * S + (which == 2 ? S - 8 : 0));
      unsigned __int128 w = ((unsigned __int128)(((u64)t[3] << 32) | t[2]) << 64) | (((u64)t[1] << 32) | t[0]);
      if (which == 0 || (which == 1 && sg == 0)) w = 0;
      else if (which == 1) w <<= 16 * (8 - sg);
      else w >>= 16 * sg;
      *(LAS u32x4*)(sm + L_KC + ci * KC_ST + (which == 2 ? 257 : which) * 16) = (u32x4){(u32)w, (u32)(w >> 32), (u32)(w >> 64), (u32)(w >> 96)};
    }
    tag = make_tag((u32x2){(u32)raw, (u32)(raw >> 32)});
    asm volatile("s_waitcnt lgkmcnt(0)" ::: "memory");
    STAMP(1);
    const int ni = wave == 3 ? 2 : 4;
    for (int i = 0; i < ni; i++) {
      const int n = 64 * i + lane, r = n >> 2, c = n & 3;
      st16_sc1(ws + WS_REC, (T * NP + wave * 64) * 64 + n * 16, *(LAS u32x4*)(wl + 64 * r + 16 * (c ^ ((r >> 1) & 3))));
    }
    WAIT_VM(0);
    STAMP(2);
    if (lane < 3 && 2 * wave + lane < 8) st8_sc1(ws + WS_F0, (((2 * wave + lane) * 4 + wave) * 256 + T) * 16, tag);
    if (!worker) { STAMP_OUT(); return; }
  } else {
    const int w4 = wave - 4, s = s0 + 2 * w4;
    u32x4 xa, xb;
    u32x2 raw;
    asm volatile("global_load_dwordx4 %0, %3, %4\n\tglobal_load_dwordx4 %1, %3, %4 offset:896\n\tglobal_load_dwordx2 %2, %5, %6 sc1"
                 : "=&v"(xa), "=&v"(xb), "=&v"(raw) : "v"((lane < 56 ? lane : 0) * 16), "s"(x + s * NP), "v"(0), "s"(ws + WS_NONCE) : "memory");
    if (worker) {
#pragma unroll
      for (int ch = 0; ch < 2; ch++) {
        const char* kr = (const char*)(ssmk + (size_t)(2 * p + ch) * S + w4);
#pragma unroll
        for (int i = 0; i < 4; i++)
          if (i < 3 || lane < 63) glds(kr + 1024 * i, lane * 16, L_KC + (2 * w4 + ch) * KC_ST + 32 + 1024 * i);
      }
      stage_w1<0, 12>(wg, wu, fs, w4, lane);
      if (w4 < 2) *(LAS u32x4*)(sm + L_REC + 16384 + (w4 * 64 + lane) * 16) = (u32x4){0, 0, 0, 0};
    } else WAIT_VM(0);
    asm volatile("s_waitcnt vmcnt(32)" : "+v"(xa), "+v"(xb), "+v"(raw) :: "memory");
    tag = make_tag(raw);
    STAMP(1);
#pragma unroll
    for (int i = 0; i < 2; i++) {
      const u32x4 d = i ? xb : xa;
      float f[8], sum = 0, sq = 0;
#pragma unroll
      for (int j = 0; j < 4; j++) { f[2 * j] = bflo(d[j]); f[2 * j + 1] = bfhi(d[j]); }
#pragma unroll
      for (int j = 0; j < 8; j++) sum += f[j];
      const float mean = wave_sum(lane < 56 ? sum : 0.0f) * (1.0f / D);
#pragma unroll
      for (int j = 0; j < 8; j++) { const float t = f[j] - mean; sq = fmaf(t, t, sq); }
      const float var = wave_sum(lane < 56 ? sq : 0.0f) * (1.0f / D);
      if (lane == 0) st8_sc1(ws + WS_STAT, (s + i) * 8, (u32x2){pk2(mean, ln_den(var)), tag[0]});
    }
    if (!worker) { STAMP_OUT(); return; }
    WAIT_VM(0);
    STAMP(2);
  }
  bf16x8 W[28];
  f32x16 cs = {};
  const float d0 = bflo(((const u32*)ssmd)[p]), d1 = bfhi(((const u32*)ssmd)[p]);
  BARRIER();
  STAMP(3);
  read_w1<0, 12>(sm, W, cs, wave, lane, true);
  BARRIER();
  if (wave >= 4) stage_w1<12, 8>(wg, wu, fs, wave - 4, lane);
  colsum<0, 12>(W, cs);
  if (wave < 4) {
    STAMP(4);
    poll(ws + WS_F0, ((xc * 4 + (p >> 6)) * 256 + 255 - (wave * 64 + lane)) * 16, tag);
    STAMP(5);
#pragma unroll
    for (int i = 0; i < 4; i++) {
      const int g = wave * 64 + i * 16 + (lane >> 2);
      glds_sc1(ws + WS_REC, ((255 - g) * NP + p) * 64 + (((lane & 3) - (g >> 2)) & 3) * 16, L_REC + (wave * 4 + i) * 1024);
    }
  } else WAIT_VM(0);
  BARRIER();
  STAMP(6);
  read_w1<12, 8>(sm, W, cs, wave, lane, true);
  WAIT_VM(0);
  BARRIER();
  STAMP(7);
  if (wave >= 4) stage_w1<20, 8>(wg, wu, fs, wave - 4, lane);
  const int ch = wave >> 2, bq = wave & 3;
  conv_mma<0>(sm, ch, bq, lane);
  BARRIER();
  if (wave < 4) conv_fin<0>(sm, ws, p, bq, lane, d0, d1, tag[0]);
  STAMP(8);
  conv_mma<1>(sm, ch, bq, lane);
  if (wave < 4) conv_pub<0>(ws, p, bq, lane, tag);
  STAMP(9);
  BARRIER();
  if (wave >= 4) conv_fin<1>(sm, ws, p, bq, lane, d0, d1, tag[0]);
  colsum<12, 8>(W, cs);
  conv_mma<2>(sm, ch, bq, lane);
  if (wave >= 4) conv_pub<1>(ws, p, bq, lane, tag);
  STAMP(10);
  BARRIER();
  if (wave < 4) conv_fin<2>(sm, ws, p, bq, lane, d0, d1, tag[0]);
  read_w1<20, 8>(sm, W, cs, wave, lane, true);
  conv_mma<3>(sm, ch, bq, lane);
  if (wave < 4) conv_pub<2>(ws, p, bq, lane, tag);
  STAMP(11);
  BARRIER();
  if (wave >= 4) { conv_fin<3>(sm, ws, p, bq, lane, d0, d1, tag[0]); conv_pub<3>(ws, p, bq, lane, tag); }
  else load_h<0>(ws, tg, xc >> 2, wave, lane, tag);
  STAMP(12);
  colsum<20, 8>(W, cs);
  const int tg2 = 2 * xc + li / 14, th = (li % 14) / 7, ds = li % 7;
  const int ft = fs * 8 + wave;
  f32x16 accA, accB;
  WAIT_VM(0);
  BARRIER();
  STAMP(13);
  if (wave < 4) load_h<1>(ws, tg, xc >> 2, wave, lane, tag);
  else {
#pragma unroll
    for (int i = 0; i < 6; i++) stage_wd(wd, ds, 2 * (wave - 4) + i / 3, i % 3, lane, L_WD + (2 * (wave - 4) + i / 3) * WD_ST + (i % 3) * 4096);
  }
  g1_stats<0>(sm, wave, lane);
  g1_chain<0>(sm, W, accA, lane);
  WAIT_VM(0);
  BARRIER();
  STAMP(14);
  if (wave < 4) load_h<2>(ws, tg, xc >> 2, wave, lane, tag);
  g1_stats<1>(sm, wave, lane);
  { g1_chain<1>(sm, W, accB, lane); u32x4 o = g1_epi<0>(sm, accA, cs, lane); g1_store<0>(ws, tg, ft, lane, o); }
  WAIT_VM(0);
  g1_flag<0>(ws, tg, ft, lane, tag);
  BARRIER();
  STAMP(15);
  if (wave < 4) load_h<3>(ws, tg, xc >> 2, wave, lane, tag);
  g1_stats<2>(sm, wave, lane);
  { g1_chain<2>(sm, W, accA, lane); u32x4 o = g1_epi<1>(sm, accB, cs, lane); g1_store<1>(ws, tg, ft, lane, o); }
  WAIT_VM(0);
  g1_flag<1>(ws, tg, ft, lane, tag);
  BARRIER();
  STAMP(16);
  g1_stats<3>(sm, wave, lane);
  { g1_chain<3>(sm, W, accB, lane); u32x4 o = g1_epi<2>(sm, accA, cs, lane); g1_store<2>(ws, tg, ft, lane, o); }
  BARRIER();
  STAMP(17);
  { u32x4 o = g1_epi<3>(sm, accB, cs, lane); g1_store<3>(ws, tg, ft, lane, o); }
  bf16x8 Wd[7][4];
  lptr wdl = sm + L_WD + wave * WD_ST;
#pragma unroll
  for (int i = 0; i < 3; i++) read_wd(wdl + i * 4096, Wd[i], lane);
  WAIT_VM(0);
  g1_flag<2>(ws, tg, ft, lane, tag);
  g1_flag<3>(ws, tg, ft, lane, tag);
  STAMP(18);
  BARRIER();
#pragma unroll
  for (int i = 0; i < 4; i++) stage_wd(wd, ds, wave, 3 + i, lane, i < 3 ? L_WD + wave * WD_ST + i * 4096 : wave * 4096);
  WAIT_VM(0);
#pragma unroll
  for (int i = 0; i < 4; i++) read_wd(i < 3 ? wdl + i * 4096 : sm + wave * 4096, Wd[3 + i], lane);
  asm volatile("s_waitcnt lgkmcnt(0)" ::: "memory");
  STAMP(19);
  u32x4 id;
  {
    const int j = (lane & 15) + 16 * (wave & 1) - 8 * (lane >> 4);
#pragma unroll
    for (int e = 0; e < 4; e++) id[e] = (j == 2 * e ? 0x3f80u : 0u) | (j == 2 * e + 1 ? 0x3f800000u : 0u);
  }
#pragma unroll
  for (int u = 0; u < 4; u++) {
    const int tt = 16 * u + tg2;
    poll(ws + WS_FG, (tt * 128 + 14 * wave + (lane < 14 ? lane : 0)) * 16, tag);
    if (u == 0) STAMP(20); if (u == 1) STAMP(22); if (u == 2) STAMP(24); if (u == 3) STAMP(26);
    if (wave < 4) {
      const char* hb = ws + WS_HT + tt * H_BYTES + (64 * ds + 32 * (wave >> 1)) * 64;
      glds_sc1(hb, lane * 16, L_WD + wave * WD_ST + u * 2048);
      glds_sc1(hb + 1024, lane * 16, L_WD + wave * WD_ST + u * 2048 + 1024);
    }
    const __amdgpu_buffer_rsrc_t ra = __builtin_amdgcn_make_buffer_rsrc(ws + WS_ACT, 0, S * F * 2, 0x00020000);
    bf16x8 B[7];
#pragma unroll
    for (int i = 0; i < 7; i++) B[i] = __builtin_bit_cast(bf16x8, __builtin_amdgcn_raw_buffer_load_b128(ra, ((tt * 2 + th) * 112 + 14 * wave + 2 * i) * 512 + lane * 16, 0, 16));
    f32x4 acc[4] = {};
#pragma unroll
    for (int i = 0; i < 7; i++)
#pragma unroll
      for (int dt = 0; dt < 4; dt++) acc[dt] = __builtin_amdgcn_mfma_f32_16x16x32_bf16(Wd[i][dt], B[i], acc[dt], 0, 0, 0);
    if (wave < 4) {
      WAIT_VM(0);
      lptr hb = wdl + u * 2048 + (8 * (lane >> 4) + ((lane & 15) >> 2)) * 64 + th * 32 + (lane & 3) * 8;
      const bf16x8 hf = cat8(trd(hb), trd(hb + 256));
      const f32x4 z = {};
      const f32x4 r = __builtin_amdgcn_mfma_f32_16x16x32_bf16(__builtin_bit_cast(bf16x8, id), hf, z, 0, 0, 0);
#pragma unroll
      for (int dt = 0; dt < 4; dt++) acc[dt] += wave == dt ? r : z;
    }
    const int tok = lane & 15;
    lptr red = sm + ((u & 1) ? 0 : L_RED2);
#pragma unroll
    for (int dt = 0; dt < 4; dt++) *(LAS f32x4*)(red + wave * 4096 + tok * 256 + 16 * ((4 * dt + (lane >> 4)) ^ tok)) = acc[dt];
    BARRIER();
    if ((wave >> 2) == (u & 1)) {
      const int tk = (tid & 255) >> 4, sl = tid & 15;
      lptr rp = red + tk * 256 + 16 * (sl ^ tk);
      f32x4 o = *(LAS f32x4*)rp;
#pragma unroll
      for (int w = 1; w < 8; w++) o += *(LAS f32x4*)(rp + w * 4096);
      *(u32x2*)(out + (size_t)(32 * tt + 16 * th + tk) * D + 64 * ds + 4 * sl) = (u32x2){pk2(o[0], o[1]), pk2(o[2], o[3])};
    }
    if (u == 0) STAMP(21); if (u == 1) STAMP(23); if (u == 2) STAMP(25); if (u == 3) STAMP(27);
  }
  if (bx == 0 && tid == 0) st8_sc1(ws + WS_NONCE, 0, tag);
  STAMP_OUT();
}

extern "C" void kernel_launch(void* const* d_in, const int* in_sizes, int n_in, void* d_out, int out_size, void* d_ws, size_t ws_size,
                              hipStream_t stream) {
  k_fused<<<256, 512, 0, stream>>>((const u32*)d_in[0], (const u32*)d_in[1], (const u32*)d_in[2], (const u32*)d_in[3], (const u16*)d_in[4],
                                   (const u16*)d_in[5], (const u16*)d_in[7], (const u16*)d_in[8], (const u16*)d_in[9], (char*)d_ws, (u16*)d_out);
}
```

```cpp
#include <hip/hip_runtime.h>
constexpr unsigned WS_OFF = 0, WS_DBG = 18167808, WS_REC = 0, WS_STAT = 3670016, WS_HS = 3686400, WS_HT = 3719168, WS_ACT = 5554176;

__global__ void __launch_bounds__(512, 2)
k_fused(const void* x, const void* k, const void* v, const void* shk, const void* ssmk, const void* ssmd, const void* wg, const void* wu,
        const void* wd, void* ws, void* out) {
  __shared__ __attribute__((aligned(256))) char smem[163840];
  unsigned tid = threadIdx.x;
  asm volatile(
    ".Lk_begin:\n"
    "s_load_dwordx16 s[4:19], %1, 0x0\n"
    "s_load_dwordx4 s[20:23], %1, 0x40\n"
    "s_load_dwordx2 s[24:25], %1, 0x50\n"
    "v_lshrrev_b32 v6, 6, v0\n"
    "v_and_b32 v1, 63, v0\n"
    "v_readfirstlane_b32 s28, v6\n"
    "v_lshlrev_b32 v2, 4, v1\n"
    "v_mov_b32 v3, 0\n"
    "s_and_b32 s26, %2, 7\n"
    "s_lshr_b32 s27, %2, 3\n"
    "s_lshl_b32 s90, s26, 5\n"
    "s_add_u32 s90, s90, s27\n"
    "s_mul_i32 s29, s26, 28\n"
    "s_add_u32 s29, s29, s27\n"
    "s_mul_i32 s54, s90, 7168\n"
    "s_and_b32 s91, s28, 3\n"
    "s_waitcnt lgkmcnt(0)\n"
    "s_cmp_ge_u32 s28, 4\n"
    "s_cbranch_scc1 .Lk_B0\n"
    "s_setprio 3\n"
    "s_add_u32 s46, s6, s54\n"
    "s_addc_u32 s47, s7, 0\n"
    "s_add_u32 s48, s8, s54\n"
    "s_addc_u32 s49, s9, 0\n"
    "s_add_u32 s50, s4, s54\n"
    "s_addc_u32 s51, s5, 0\n"
    "s_add_u32 s52, s22, 18163712\n"
    "s_addc_u32 s53, s23, 0\n"
    "v_lshlrev_b32 v134, 2, v0\n"
    "v_lshlrev_b32 v136, 4, v0\n"
    "v_add_u32 v135, 4480, v134\n"
    "v_cmp_gt_u32 vcc, 224, v0\n"
    "s_and_b64 exec, exec, vcc\n"
    "s_cmp_eq_u32 s90, 0\n"
    "s_cbranch_scc1 .Lk_t0\n"
    "global_load_dword v32, v134, s[46:47] offset:-2688 nt\n"
    "global_load_dword v33, v134, s[46:47] offset:-1792 nt\n"
    "global_load_dword v34, v134, s[46:47] offset:-896 nt\n"
    ".Lk_t0b:\n"
    "global_load_dwordx4 v[60:63], v136, s[10:11]\n"
    "global_load_dword v35, v134, s[46:47] offset:0 nt\n"
    "global_load_dword v44, v134, s[48:49] offset:0 nt\n"
    "global_load_dword v36, v134, s[46:47] offset:896 nt\n"
    "global_load_dword v45, v134, s[48:49] offset:896 nt\n"
    "global_load_dword v37, v134, s[46:47] offset:1792 nt\n"
    "global_load_dword v46, v134, s[48:49] offset:1792 nt\n"
    "global_load_dword v38, v134, s[46:47] offset:2688 nt\n"
    "global_load_dword v47, v134, s[48:49] offset:2688 nt\n"
    "global_load_dword v39, v134, s[46:47] offset:3584 nt\n"
    "global_load_dword v48, v134, s[48:49] offset:3584 nt\n"
    "global_load_dword v40, v135, s[46:47] offset:0 nt\n"
    "global_load_dword v49, v135, s[48:49] offset:0 nt\n"
    "global_load_dword v41, v135, s[46:47] offset:896 nt\n"
    "global_load_dword v50, v135, s[48:49] offset:896 nt\n"
    "global_load_dword v42, v135, s[46:47] offset:1792 nt\n"
    "global_load_dword v51, v135, s[48:49] offset:1792 nt\n"
    "global_load_dword v52, v134, s[50:51] offset:0 nt\n"
    "global_load_dword v53, v134, s[50:51] offset:896 nt\n"
    "global_load_dword v54, v134, s[50:51] offset:1792 nt\n"
    "global_load_dword v55, v134, s[50:51] offset:2688 nt\n"
    "global_load_dword v56, v134, s[50:51] offset:3584 nt\n"
    "global_load_dword v57, v135, s[50:51] offset:0 nt\n"
    "global_load_dword v58, v135, s[50:51] offset:896 nt\n"
    "global_load_dword v59, v135, s[50:51] offset:1792 nt\n"
    "global_load_dwordx2 v[64:65], v3, s[52:53] sc1\n"
    "s_mov_b64 exec, -1\n"
    "s_memrealtime s[60:61]\n"
    "s_min_u32 s55, s29, 223\n"
    "s_lshl_b32 s55, s55, 2\n"
    "s_load_dword s36, s[14:15], s55\n"
    "s_and_b32 s55, s26, 3\n"
    "s_and_b32 s56, s27, 3\n"
    "s_lshl2_add_u32 s30, s55, s56\n"
    "s_lshr_b32 s41, s26, 2\n"
    "s_mul_i32 s31, s41, 7\n"
    "s_lshr_b32 s56, s27, 2\n"
    "s_add_u32 s31, s31, s56\n"
    "s_lshl_b32 s55, s55, 1\n"
    "s_add_u32 s55, s55, s41\n"
    "s_lshl_b32 s55, s55, 12\n"
    "s_add_u32 s55, s55, 13025280\n"
    "s_add_u32 s78, s22, s55\n"
    "s_addc_u32 s79, s23, 0\n"
    "s_mul_i32 s55, s30, 28672\n"
    "s_add_u32 s55, s55, 3719168\n"
    "s_add_u32 s80, s22, s55\n"
    "s_addc_u32 s81, s23, 0\n"
    "s_lshl_b32 s55, s41, 14\n"
    "s_lshl_b32 s56, s30, 8\n"
    "s_add_u32 s55, s55, s56\n"
    "s_add_u32 s55, s55, 3686400\n"
    "s_add_u32 s82, s22, s55\n"
    "s_addc_u32 s83, s23, 0\n"
    "s_mov_b32 s64, 0x05040100\n"
    "s_mov_b32 s65, 0x07060302\n"
    "s_lshl_b32 s55, s28, 12\n"
    "s_add_u32 s55, s55, 131584\n"
    "v_bfe_u32 v134, v1, 1, 2\n"
    "v_lshlrev_b32 v134, 4, v134\n"
    "v_lshl_or_b32 v134, v1, 6, v134\n"
    "v_add_u32 v134, s55, v134\n"
    "v_xor_b32 v135, 16, v134\n"
    "v_xor_b32 v136, 32, v134\n"
    "v_xor_b32 v137, 48, v134\n"
    "v_bfe_u32 v6, v1, 3, 2\n"
    "v_xor_b32 v6, v6, v1\n"
    "v_and_b32 v6, 3, v6\n"
    "v_lshlrev_b32 v6, 4, v6\n"
    "v_lshrrev_b32 v7, 2, v1\n"
    "v_lshl_or_b32 v6, v7, 6, v6\n"
    "v_add_u32 v138, s55, v6\n"
    "s_mul_i32 s56, s90, 14336\n"
    "s_lshl_b32 s57, s28, 12\n"
    "s_add_u32 s56, s56, s57\n"
    "s_add_u32 s92, s22, s56\n"
    "s_addc_u32 s93, s23, 0\n"
    "s_mul_i32 s56, s28, 2304\n"
    "s_add_u32 s56, s56, s90\n"
    "s_lshl_b32 s56, s56, 4\n"
    "s_add_u32 s56, s56, 12894208\n"
    "s_add_u32 s50, s22, s56\n"
    "s_addc_u32 s51, s23, 0\n"
    "s_cmp_eq_u32 s28, 3\n"
    "s_cselect_b64 s[62:63], 3, 7\n"
    "v_lshlrev_b32 v140, 14, v1\n"
    "v_min_u32 v8, 27, v1\n"
    "v_lshlrev_b32 v8, 4, v8\n"
    "v_and_b32 v6, 15, v1\n"
    "v_lshrrev_b32 v7, 4, v1\n"
    "v_add_u32 v22, 1, v6\n"
    "v_and_b32 v10, 3, v22\n"
    "v_mul_u32_u24 v10, 8256, v10\n"
    "v_and_b32 v22, 28, v22\n"
    "v_lshl_add_u32 v22, v7, 3, v22\n"
    "v_lshl_add_u32 v10, v22, 1, v10\n"
    "s_lshl_b32 s55, s91, 7\n"
    "s_add_u32 s55, s55, 98560\n"
    "v_add_u32 v10, s55, v10\n"
    "s_lshl_b32 s55, s91, 3\n"
    "s_add_u32 s55, s55, 30\n"
    "v_lshlrev_b32 v22, 1, v6\n"
    "v_sub_u32 v22, v7, v22\n"
    "v_add_u32 v22, s55, v22\n"
    "v_lshrrev_b32 v11, 2, v22\n"
    "v_add_u32 v11, 0, v11\n"
    "v_and_b32 v11, 3, v11\n"
    "v_lshlrev_b32 v11, 4, v11\n"
    "v_lshl_or_b32 v11, v22, 6, v11\n"
    "v_add_u32 v11, 131584, v11\n"
    "v_add_u32 v22, 4, v22\n"
    "v_lshrrev_b32 v12, 2, v22\n"
    "v_add_u32 v12, 0, v12\n"
    "v_and_b32 v12, 3, v12\n"
    "v_lshlrev_b32 v12, 4, v12\n"
    "v_lshl_or_b32 v12, v22, 6, v12\n"
    "v_add_u32 v12, 131584, v12\n"
    "v_lshl_add_u32 v22, v7, 2, v6\n"
    "v_and_b32 v22, 15, v22\n"
    "v_lshlrev_b32 v22, 4, v22\n"
    "v_lshl_or_b32 v22, v7, 8, v22\n"
    "s_lshl_b32 s55, s28, 11\n"
    "s_add_u32 s55, s55, 65792\n"
    "v_add_u32 v13, s55, v22\n"
    "v_and_b32 v6, 3, v1\n"
    "v_bfe_u32 v7, v1, 3, 1\n"
    "v_bfe_u32 v22, v1, 2, 1\n"
    "v_lshrrev_b32 v16, 4, v1\n"
    "v_lshl_or_b32 v22, v16, 1, v22\n"
    "s_and_b32 s55, s91, 1\n"
    "s_lshl_b32 s55, s55, 3\n"
    "v_or_b32 v22, s55, v22\n"
    "s_lshr_b32 s56, s91, 1\n"
    "v_lshl_add_u32 v14, v6, 2, v22\n"
    "v_and_b32 v14, 15, v14\n"
    "v_lshlrev_b32 v14, 4, v14\n"
    "v_lshl_or_b32 v14, v6, 8, v14\n"
    "v_lshl_or_b32 v14, v7, 13, v14\n"
    "s_lshl_b32 s57, s56, 10\n"
    "s_add_u32 s57, s57, 65792\n"
    "v_add_u32 v14, s57, v14\n"
    "v_lshlrev_b32 v18, 4, v22\n"
    "v_lshl_or_b32 v18, v6, 2, v18\n"
    "s_lshl_b32 s57, s56, 8\n"
    "v_or_b32 v18, s57, v18\n"
    "v_lshrrev_b32 v17, 5, v18\n"
    "v_mul_u32_u24 v17, 28672, v17\n"
    "v_and_b32 v15, 31, v18\n"
    "v_lshl_add_u32 v17, v15, 1, v17\n"
    "v_lshl_add_u32 v17, v7, 6, v17\n"
    "v_lshrrev_b32 v22, 3, v18\n"
    "v_sub_u32 v22, 63, v22\n"
    "v_lshlrev_b32 v18, 3, v18\n"
    "v_lshrrev_b32 v15, 2, v22\n"
    "v_add_u32 v15, v15, v7\n"
    "v_and_b32 v15, 3, v15\n"
    "v_lshlrev_b32 v15, 4, v15\n"
    "v_lshl_or_b32 v15, v22, 6, v15\n"
    "v_and_b32 v6, 1, v6\n"
    "v_lshl_or_b32 v15, v6, 3, v15\n"
    "v_xor_b32 v16, 32, v15\n"
    "v_xor_b32 v15, 8, v15\n"
    "v_add_u32 v16, 131584, v16\n"
    "v_add_u32 v15, 131584, v15\n"
    "s_waitcnt lgkmcnt(0)\n"
    "s_mov_b32 s54, s60\n"
    "s_lshl_b32 s55, s91, 9\n"
    "s_add_u32 s55, s55, s29\n"
    "s_lshl_b32 s55, s55, 4\n"
    "s_add_u32 s55, s55, 13025280\n"
    "s_add_u32 s98, s22, s55\n"
    "s_addc_u32 s99, s23, 0\n"
    "s_lshl3_add_u32 s40, s31, s28\n"
    "s_mul_i32 s55, s30, 224\n"
    "s_add_u32 s55, s55, s40\n"
    "s_lshl_b32 s55, s55, 9\n"
    "s_add_u32 s55, s55, 5554176\n"
    "s_add_u32 s84, s22, s55\n"
    "s_addc_u32 s85, s23, 0\n"
    "s_lshl_b32 s55, s30, 7\n"
    "s_add_u32 s55, s55, s40\n"
    "s_lshl_b32 s55, s55, 4\n"
    "s_add_u32 s55, s55, 13156352\n"
    "s_add_u32 s86, s22, s55\n"
    "s_addc_u32 s87, s23, 0\n"
    "s_add_u32 s56, s26, 1\n"
    "s_and_b32 s56, s56, 3\n"
    "s_lshl1_add_u32 s56, s56, s41\n"
    "s_cmp_ge_u32 s27, 14\n"
    "s_cselect_b32 s55, 1, 0\n"
    "s_lshl1_add_u32 s37, s56, s55\n"
    "s_mul_i32 s55, s55, 14\n"
    "s_sub_u32 s55, s27, s55\n"
    "s_cmp_ge_u32 s55, 7\n"
    "s_cselect_b32 s38, 1, 0\n"
    "s_mul_i32 s56, s38, 7\n"
    "s_sub_u32 s39, s55, s56\n"
    "s_lshl3_add_u32 s55, s39, s28\n"
    "s_mul_i32 s56, s55, 28672\n"
    "s_add_u32 s56, s56, 16498688\n"
    "s_add_u32 s88, s22, s56\n"
    "s_addc_u32 s89, s23, 0\n"
    "s_lshl_b32 s55, s55, 7\n"
    "s_add_u32 s97, s55, 18118656\n"
    "s_mul_i32 s55, s40, 28672\n"
    "s_add_u32 s55, s55, 13287424\n"
    "s_add_u32 s8, s22, s55\n"
    "s_addc_u32 s9, s23, 0\n"
    "s_lshr_b32 s55, s28, 2\n"
    "s_lshl1_add_u32 s55, s31, s55\n"
    "s_lshl_b32 s55, s55, 9\n"
    "s_add_u32 s55, s55, 18104320\n"
    "s_add_u32 s66, s22, s55\n"
    "s_addc_u32 s67, s23, 0\n"
    "s_lshl_b32 s55, s28, 13\n"
    "s_cmp_lt_u32 s28, 5\n"
    "s_cselect_b32 s42, 0, 28672\n"
    "s_add_u32 s42, s42, s55\n"
    "s_add_u32 s42, s42, 57344\n"
    "s_lshl_b32 s55, s28, 12\n"
    "s_add_u32 s56, s55, 98304\n"
    "s_add_u32 s55, s55, 28672\n"
    "s_cmp_lt_u32 s28, 7\n"
    "s_cselect_b32 s43, s55, 151552\n"
    "s_cselect_b32 s44, s56, 155648\n"
    "s_mul_i32 s74, s28, 3584\n"
    "s_mul_i32 s57, s37, 28672\n"
    "s_lshl_b32 s55, s39, 12\n"
    "s_add_u32 s57, s57, s55\n"
    "s_lshl_b32 s55, s91, 10\n"
    "s_add_u32 s57, s57, s55\n"
    "s_add_u32 s57, s57, 3719168\n"
    "s_add_u32 s68, s22, s57\n"
    "s_addc_u32 s69, s23, 0\n"
    "s_lshl1_add_u32 s57, s37, s38\n"
    "s_mul_i32 s55, s57, 112\n"
    "s_mul_i32 s56, s28, 14\n"
    "s_add_u32 s55, s55, s56\n"
    "s_lshl_b32 s55, s55, 9\n"
    "s_add_u32 s55, s55, 5554176\n"
    "s_add_u32 s70, s22, s55\n"
    "s_addc_u32 s71, s23, 0\n"
    "s_mul_i32 s55, s57, 14336\n"
    "s_lshl_b32 s56, s39, 7\n"
    "s_add_u32 s55, s55, s56\n"
    "s_add_u32 s72, s24, s55\n"
    "s_addc_u32 s73, s25, 0\n"
    "s_lshl_b32 s55, s36, 16\n"
    "s_and_b32 s56, s36, 0xffff0000\n"
    "v_bfe_u32 v22, v1, 3, 1\n"
    "v_cmp_eq_u32 vcc, 1, v22\n"
    "v_mov_b32 v19, s55\n"
    "v_mov_b32 v22, s56\n"
    "v_cndmask_b32 v19, v19, v22, vcc\n"
    "s_lshr_b32 s55, s29, 6\n"
    "s_lshl2_add_u32 s55, s26, s55\n"
    "s_lshl_b32 s55, s55, 12\n"
    "s_add_u32 s55, s55, 12894208\n"
    "s_add_u32 s46, s22, s55\n"
    "s_addc_u32 s47, s23, 0\n"
    "v_sub_u32 v6, 255, v0\n"
    "v_lshlrev_b32 v6, 4, v6\n"
    "v_lshrrev_b32 v22, 4, v1\n"
    "v_sub_u32 v22, v1, v22\n"
    "v_and_b32 v22, 3, v22\n"
    "v_lshrrev_b32 v7, 2, v1\n"
    "v_sub_u32 v7, 15, v7\n"
    "v_mul_u32_u24 v7, 14336, v7\n"
    "v_lshl_or_b32 v7, v22, 4, v7\n"
    "s_lshl_b32 s55, s28, 6\n"
    "s_sub_u32 s55, 240, s55\n"
    "s_mul_i32 s55, s55, 224\n"
    "s_add_u32 s55, s55, s29\n"
    "s_lshl_b32 s55, s55, 6\n"
    "s_add_u32 s48, s22, s55\n"
    "s_addc_u32 s49, s23, 0\n"
    "s_waitcnt vmcnt(25)\n"
    "v_lshlrev_b32 v92, 16, v60\n"
    "v_lshlrev_b32 v93, 16, v62\n"
    "v_and_b32 v94, 0xffff0000, v60\n"
    "v_and_b32 v95, 0xffff0000, v62\n"
    "v_lshlrev_b32 v96, 16, v61\n"
    "v_lshlrev_b32 v97, 16, v63\n"
    "v_and_b32 v98, 0xffff0000, v61\n"
    "v_and_b32 v99, 0xffff0000, v63\n"
    "v_lshlrev_b32 v70, 16, v32\n"
    "v_and_b32 v71, 0xffff0000, v32\n"
    "v_lshlrev_b32 v72, 16, v33\n"
    "v_and_b32 v73, 0xffff0000, v33\n"
    "v_lshlrev_b32 v74, 16, v34\n"
    "v_and_b32 v75, 0xffff0000, v34\n"
    "s_waitcnt vmcnt(24)\n"
    "v_lshlrev_b32 v76, 16, v35\n"
    "v_and_b32 v77, 0xffff0000, v35\n"
    "s_waitcnt vmcnt(23)\n"
    "v_lshlrev_b32 v66, 16, v44\n"
    "v_and_b32 v67, 0xffff0000, v44\n"
    "v_pk_mul_f32 v[100:101], v[70:71], v[98:99]\n"
    "v_pk_fma_f32 v[100:101], v[72:73], v[96:97], v[100:101]\n"
    "v_pk_fma_f32 v[100:101], v[74:75], v[94:95], v[100:101]\n"
    "v_pk_fma_f32 v[100:101], v[76:77], v[92:93], v[100:101]\n"
    "v_pk_mul_f32 v[100:101], v[100:101], v[66:67]\n"
    "s_waitcnt vmcnt(22)\n"
    "v_lshlrev_b32 v78, 16, v36\n"
    "v_and_b32 v79, 0xffff0000, v36\n"
    "s_waitcnt vmcnt(21)\n"
    "v_lshlrev_b32 v66, 16, v45\n"
    "v_and_b32 v67, 0xffff0000, v45\n"
    "v_pk_mul_f32 v[102:103], v[72:73], v[98:99]\n"
    "v_pk_fma_f32 v[102:103], v[74:75], v[96:97], v[102:103]\n"
    "v_pk_fma_f32 v[102:103], v[76:77], v[94:95], v[102:103]\n"
    "v_pk_fma_f32 v[102:103], v[78:79], v[92:93], v[102:103]\n"
    "v_pk_mul_f32 v[102:103], v[102:103], v[66:67]\n"
    "s_waitcnt vmcnt(20)\n"
    "v_lshlrev_b32 v80, 16, v37\n"
    "v_and_b32 v81, 0xffff0000, v37\n"
    "s_waitcnt vmcnt(19)\n"
    "v_lshlrev_b32 v66, 16, v46\n"
    "v_and_b32 v67, 0xffff0000, v46\n"
    "v_pk_mul_f32 v[104:105], v[74:75], v[98:99]\n"
    "v_pk_fma_f32 v[104:105], v[76:77], v[96:97], v[104:105]\n"
    "v_pk_fma_f32 v[104:105], v[78:79], v[94:95], v[104:105]\n"
    "v_pk_fma_f32 v[104:105], v[80:81], v[92:93], v[104:105]\n"
    "v_pk_mul_f32 v[104:105], v[104:105], v[66:67]\n"
    "s_waitcnt vmcnt(18)\n"
    "v_lshlrev_b32 v82, 16, v38\n"
    "v_and_b32 v83, 0xffff0000, v38\n"
    "s_waitcnt vmcnt(17)\n"
    "v_lshlrev_b32 v66, 16, v47\n"
    "v_and_b32 v67, 0xffff0000, v47\n"
    "v_pk_mul_f32 v[106:107], v[76:77], v[98:99]\n"
    "v_pk_fma_f32 v[106:107], v[78:79], v[96:97], v[106:107]\n"
    "v_pk_fma_f32 v[106:107], v[80:81], v[94:95], v[106:107]\n"
    "v_pk_fma_f32 v[106:107], v[82:83], v[92:93], v[106:107]\n"
    "v_pk_mul_f32 v[106:107], v[106:107], v[66:67]\n"
    "s_waitcnt vmcnt(16)\n"
    "v_lshlrev_b32 v84, 16, v39\n"
    "v_and_b32 v85, 0xffff0000, v39\n"
    "s_waitcnt vmcnt(15)\n"
    "v_lshlrev_b32 v66, 16, v48\n"
    "v_and_b32 v67, 0xffff0000, v48\n"
    "v_pk_mul_f32 v[108:109], v[78:79], v[98:99]\n"
    "v_pk_fma_f32 v[108:109], v[80:81], v[96:97], v[108:109]\n"
    "v_pk_fma_f32 v[108:109], v[82:83], v[94:95], v[108:109]\n"
    "v_pk_fma_f32 v[108:109], v[84:85], v[92:93], v[108:109]\n"
    "v_pk_mul_f32 v[108:109], v[108:109], v[66:67]\n"
    "s_waitcnt vmcnt(14)\n"
    "v_lshlrev_b32 v86, 16, v40\n"
    "v_and_b32 v87, 0xffff0000, v40\n"
    "s_waitcnt vmcnt(13)\n"
    "v_lshlrev_b32 v66, 16, v49\n"
    "v_and_b32 v67, 0xffff0000, v49\n"
    "v_pk_mul_f32 v[110:111], v[80:81], v[98:99]\n"
    "v_pk_fma_f32 v[110:111], v[82:83], v[96:97], v[110:111]\n"
    "v_pk_fma_f32 v[110:111], v[84:85], v[94:95], v[110:111]\n"
    "v_pk_fma_f32 v[110:111], v[86:87], v[92:93], v[110:111]\n"
    "v_pk_mul_f32 v[110:111], v[110:111], v[66:67]\n"
    "s_waitcnt vmcnt(12)\n"
    "v_lshlrev_b32 v88, 16, v41\n"
    "v_and_b32 v89, 0xffff0000, v41\n"
    "s_waitcnt vmcnt(11)\n"
    "v_lshlrev_b32 v66, 16, v50\n"
    "v_and_b32 v67, 0xffff0000, v50\n"
    "v_pk_mul_f32 v[112:113], v[82:83], v[98:99]\n"
    "v_pk_fma_f32 v[112:113], v[84:85], v[96:97], v[112:113]\n"
    "v_pk_fma_f32 v[112:113], v[86:87], v[94:95], v[112:113]\n"
    "v_pk_fma_f32 v[112:113], v[88:89], v[92:93], v[112:113]\n"
    "v_pk_mul_f32 v[112:113], v[112:113], v[66:67]\n"
    "s_waitcnt vmcnt(10)\n"
    "v_lshlrev_b32 v90, 16, v42\n"
    "v_and_b32 v91, 0xffff0000, v42\n"
    "s_waitcnt vmcnt(9)\n"
    "v_lshlrev_b32 v66, 16, v51\n"
    "v_and_b32 v67, 0xffff0000, v51\n"
    "v_pk_mul_f32 v[114:115], v[84:85], v[98:99]\n"
    "v_pk_fma_f32 v[114:115], v[86:87], v[96:97], v[114:115]\n"
    "v_pk_fma_f32 v[114:115], v[88:89], v[94:95], v[114:115]\n"
    "v_pk_fma_f32 v[114:115], v[90:91], v[92:93], v[114:115]\n"
    "v_pk_mul_f32 v[114:115], v[114:115], v[66:67]\n"
    "v_cvt_pk_bf16_f32 v116, v114, v112\n"
    "v_cvt_pk_bf16_f32 v117, v110, v108\n"
    "v_cvt_pk_bf16_f32 v118, v106, v104\n"
    "v_cvt_pk_bf16_f32 v119, v102, v100\n"
    "ds_write_b128 v134, v[116:119]\n"
    "v_cvt_pk_bf16_f32 v120, v115, v113\n"
    "v_cvt_pk_bf16_f32 v121, v111, v109\n"
    "v_cvt_pk_bf16_f32 v122, v107, v105\n"
    "v_cvt_pk_bf16_f32 v123, v103, v101\n"
    "ds_write_b128 v135, v[120:123]\n"
    "s_waitcnt vmcnt(7)\n"
    "v_perm_b32 v124, v53, v52, s64\n"
    "s_waitcnt vmcnt(5)\n"
    "v_perm_b32 v125, v55, v54, s64\n"
    "s_waitcnt vmcnt(3)\n"
    "v_perm_b32 v126, v57, v56, s64\n"
    "s_waitcnt vmcnt(1)\n"
    "v_perm_b32 v127, v59, v58, s64\n"
    "ds_write_b128 v136, v[124:127]\n"
    "v_perm_b32 v128, v53, v52, s65\n"
    "v_perm_b32 v129, v55, v54, s65\n"
    "v_perm_b32 v130, v57, v56, s65\n"
    "v_perm_b32 v131, v59, v58, s65\n"
    "ds_write_b128 v137, v[128:131]\n"
    "ds_read_b128 v[116:119], v138 offset:0\n"
    "ds_read_b128 v[120:123], v138 offset:1024\n"
    "ds_read_b128 v[124:127], v138 offset:2048\n"
    "ds_read_b128 v[128:131], v138 offset:3072\n"
    "s_waitcnt vmcnt(0)\n"
    "v_readfirstlane_b32 s34, v64\n"
    "v_readfirstlane_b32 s35, v65\n"
    "s_add_u32 s34, s34, 0x7F4A7C15\n"
    "s_addc_u32 s35, s35, 0x9E3779B9\n"
    "v_mov_b32 v4, s34\n"
    "v_mov_b32 v5, s35\n"
    "s_waitcnt lgkmcnt(3)\n"
    "global_store_dwordx4 v2, v[116:119], s[92:93] offset:0 sc1\n"
    "s_waitcnt lgkmcnt(2)\n"
    "global_store_dwordx4 v2, v[120:123], s[92:93] offset:1024 sc1\n"
    "s_cmp_eq_u32 s28, 3\n"
    "s_cbranch_scc1 .Lk_wave3\n"
    "s_waitcnt lgkmcnt(1)\n"
    "global_store_dwordx4 v2, v[124:127], s[92:93] offset:2048 sc1\n"
    "s_waitcnt lgkmcnt(0)\n"
    "global_store_dwordx4 v2, v[128:131], s[92:93] offset:3072 sc1\n"
    ".Lk_wave3:\n"
    "s_waitcnt lgkmcnt(0)\n"
    "s_waitcnt vmcnt(0)\n"
    "s_mov_b64 exec, s[62:63]\n"
    "global_store_dwordx2 v140, v[4:5], s[50:51] sc1\n"
    "s_mov_b64 exec, -1\n"
    "s_cmp_ge_u32 s27, 28\n"
    "s_cbranch_scc1 .Lk_helper\n"
    "s_mov_b32 s45, 4096\n"
    ".Lk_w1:\n"
    "s_memrealtime s[60:61]\n"
    "s_waitcnt lgkmcnt(0)\n"
    "s_sub_u32 s45, s45, 1\n"
    "s_cbranch_scc1 .Lk_x2\n"
    "s_sub_u32 s60, s60, s54\n"
    "s_cmp_lt_u32 s60, 330\n"
    "s_cbranch_scc1 .Lk_w1\n"
    ".Lk_x2:\n"
    "global_load_dwordx2 v[32:33], v6, s[46:47] sc1\n"
    "s_waitcnt vmcnt(0)\n"
    "v_cmp_ne_u64 vcc, s[34:35], v[32:33]\n"
    "s_cbranch_vccnz .Lk_r3\n"
    ".Lk_c4:\n"
    "s_add_u32 s50, s22, 3670016\n"
    "s_addc_u32 s51, s23, 0\n"
    "global_load_dwordx4 v[240:243], v18, s[50:51] offset:0 sc1\n"
    "global_load_dwordx4 v[244:247], v18, s[50:51] offset:16 sc1\n"
    "s_lshl_b32 s56, s28, 12\n"
    "s_add_u32 m0, s56, 131584\n"
    "s_nop 0\n"
    "global_load_lds_dwordx4 v7, s[48:49] offset:0 sc1\n"
    "s_sub_u32 s48, s48, 229376\n"
    "s_subb_u32 s49, s49, 0\n"
    "s_add_u32 m0, s56, 132608\n"
    "s_nop 0\n"
    "global_load_lds_dwordx4 v7, s[48:49] offset:0 sc1\n"
    "s_sub_u32 s48, s48, 229376\n"
    "s_subb_u32 s49, s49, 0\n"
    "s_add_u32 m0, s56, 133632\n"
    "s_nop 0\n"
    "global_load_lds_dwordx4 v7, s[48:49] offset:0 sc1\n"
    "s_sub_u32 s48, s48, 229376\n"
    "s_subb_u32 s49, s49, 0\n"
    "s_add_u32 m0, s56, 134656\n"
    "s_nop 0\n"
    "global_load_lds_dwordx4 v7, s[48:49] offset:0 sc1\n"
    "s_setprio 0\n"
    "s_waitcnt vmcnt(5)\n"
    "v_bitop3_b32 v241, v241, v243, s34 bitop3:0x7e\n"
    "s_waitcnt vmcnt(4)\n"
    "v_bitop3_b32 v245, v245, v247, s34 bitop3:0x7e\n"
    "v_or_b32 v241, v241, v245\n"
    "v_cmp_ne_u32 vcc, 0, v241\n"
    "s_cbranch_vccnz .Lk_r6\n"
    ".Lk_c7:\n"
    "v_and_b32 v241, 0xffff0000, v240\n"
    "v_and_b32 v243, 0xffff0000, v242\n"
    "v_and_b32 v245, 0xffff0000, v244\n"
    "v_and_b32 v247, 0xffff0000, v246\n"
    "v_rcp_f32 v241, v241\n"
    "v_lshlrev_b32 v240, 16, v240\n"
    "v_rcp_f32 v243, v243\n"
    "v_lshlrev_b32 v242, 16, v242\n"
    "v_rcp_f32 v245, v245\n"
    "v_lshlrev_b32 v244, 16, v244\n"
    "v_rcp_f32 v247, v247\n"
    "v_lshlrev_b32 v246, 16, v246\n"
    "s_waitcnt vmcnt(0)\n"
    "s_barrier\n"
    "ds_read_b128 v[36:39], v11 offset:14336\n"
    "ds_read_b64 v[32:33], v10 offset:0\n"
    "ds_read_b64 v[34:35], v10 offset:8\n"
    "ds_read_b128 v[44:47], v11 offset:12288\n"
    "ds_read_b64 v[40:41], v10 offset:512\n"
    "ds_read_b64 v[42:43], v10 offset:520\n"
    "ds_read_b128 v[84:87], v12 offset:14336\n"
    "ds_read_b64 v[80:81], v10 offset:64\n"
    "ds_read_b64 v[82:83], v10 offset:72\n"
    "ds_read_b128 v[92:95], v12 offset:12288\n"
    "ds_read_b64 v[88:89], v10 offset:576\n"
    "ds_read_b64 v[90:91], v10 offset:584\n"
    "ds_read_b64 v[252:253], v15 offset:12288\n"
    "ds_read_b64 v[254:255], v16 offset:12288\n"
    "global_load_dwordx2 v[20:21], v8, s[66:67] sc1\n"
    "s_waitcnt lgkmcnt(10)\n"
    "v_mfma_f32_16x16x32_bf16 v[228:231], v[32:35], v[44:47], 0\n"
    "v_mfma_f32_16x16x32_bf16 v[224:227], v[32:35], v[36:39], 0\n"
    "s_waitcnt lgkmcnt(8)\n"
    "v_mfma_f32_16x16x32_bf16 v[228:231], v[40:43], v[36:39], v[228:231]\n"
    "s_waitcnt lgkmcnt(4)\n"
    "v_mfma_f32_16x16x32_bf16 v[228:231], v[80:83], v[92:95], v[228:231]\n"
    "v_mfma_f32_16x16x32_bf16 v[224:227], v[80:83], v[84:87], v[224:227]\n"
    "s_waitcnt lgkmcnt(2)\n"
    "v_mfma_f32_16x16x32_bf16 v[228:231], v[88:91], v[84:87], v[228:231]\n"
    "s_nop 5\n"
    "ds_write_b128 v13, v[224:227] offset:0\n"
    "s_nop 0\n"
    "ds_write_b128 v13, v[228:231] offset:1024\n"
    "ds_read_b128 v[52:55], v11 offset:10240\n"
    "ds_read_b64 v[48:49], v10 offset:1024\n"
    "ds_read_b64 v[50:51], v10 offset:1032\n"
    "ds_read_b128 v[60:63], v11 offset:8192\n"
    "ds_read_b64 v[56:57], v10 offset:1536\n"
    "ds_read_b64 v[58:59], v10 offset:1544\n"
    "ds_read_b128 v[100:103], v12 offset:10240\n"
    "ds_read_b64 v[96:97], v10 offset:1088\n"
    "ds_read_b64 v[98:99], v10 offset:1096\n"
    "ds_read_b128 v[108:111], v12 offset:8192\n"
    "ds_read_b64 v[104:105], v10 offset:1600\n"
    "ds_read_b64 v[106:107], v10 offset:1608\n"
    "s_waitcnt lgkmcnt(14)\n"
    "v_lshlrev_b32 v248, 16, v254\n"
    "v_and_b32 v249, 0xffff0000, v254\n"
    "v_lshlrev_b32 v250, 16, v255\n"
    "v_and_b32 v251, 0xffff0000, v255\n"
    "v_sub_f32 v240, v248, v240\n"
    "v_sub_f32 v242, v249, v242\n"
    "v_sub_f32 v244, v250, v244\n"
    "v_sub_f32 v246, v251, v246\n"
    "v_mul_f32 v240, v240, v241\n"
    "v_mul_f32 v241, v242, v243\n"
    "v_mul_f32 v242, v244, v245\n"
    "v_mul_f32 v243, v246, v247\n"
    "v_and_b32 v244, 0xffff0000, v253\n"
    "v_lshlrev_b32 v245, 16, v253\n"
    "v_and_b32 v246, 0xffff0000, v252\n"
    "v_lshlrev_b32 v247, 16, v252\n"
    "v_mul_f32 v244, v244, v19\n"
    "v_mul_f32 v245, v245, v19\n"
    "v_mul_f32 v246, v246, v19\n"
    "v_mul_f32 v247, v247, v19\n"
    "s_lshl_b32 s55, s29, 7\n"
    "s_add_u32 s55, s55, 3719168\n"
    "s_add_u32 s46, s22, s55\n"
    "s_addc_u32 s47, s23, 0\n"
    "s_waitcnt lgkmcnt(12)\n"
    "s_barrier\n"
    "ds_read_b128 v[252:255], v14 offset:0\n"
    "ds_read_b128 v[224:227], v14 offset:2048\n"
    "ds_read_b128 v[228:231], v14 offset:4096\n"
    "ds_read_b128 v[28:31], v14 offset:6144\n"
    "s_waitcnt lgkmcnt(3)\n"
    "v_pk_add_f32 v[252:253], v[252:253], v[244:245]\n"
    "v_pk_add_f32 v[254:255], v[254:255], v[246:247]\n"
    "s_waitcnt lgkmcnt(2)\n"
    "v_pk_add_f32 v[252:253], v[252:253], v[224:225]\n"
    "v_pk_add_f32 v[254:255], v[254:255], v[226:227]\n"
    "s_waitcnt lgkmcnt(1)\n"
    "v_pk_add_f32 v[252:253], v[252:253], v[228:229]\n"
    "v_pk_add_f32 v[254:255], v[254:255], v[230:231]\n"
    "s_waitcnt lgkmcnt(0)\n"
    "v_pk_add_f32 v[252:253], v[252:253], v[28:29]\n"
    "v_pk_add_f32 v[254:255], v[254:255], v[30:31]\n"
    "v_pk_fma_f32 v[248:249], v[240:241], v[252:253], v[248:249]\n"
    "v_pk_fma_f32 v[250:251], v[242:243], v[254:255], v[250:251]\n"
    "v_cvt_pk_bf16_f32 v24, v248, v249\n"
    "v_cvt_pk_bf16_f32 v25, v250, v251\n"
    "global_store_dwordx2 v17, v[24:25], s[46:47] sc1\n"
    "s_mov_b32 s75, 0\n"
    "s_add_u32 s50, s22, 3678208\n"
    "s_addc_u32 s51, s23, 0\n"
    "global_load_dwordx4 v[240:243], v18, s[50:51] offset:0 sc1\n"
    "global_load_dwordx4 v[244:247], v18, s[50:51] offset:16 sc1\n"
    "v_mfma_f32_16x16x32_bf16 v[232:235], v[40:43], v[44:47], 0\n"
    "s_waitcnt vmcnt(3)\n"
    "v_cmp_ne_u64 vcc, s[34:35], v[20:21]\n"
    "s_cbranch_vccnz .Lk_r9\n"
    ".Lk_c10:\n"
    "v_mfma_f32_16x16x32_bf16 v[232:235], v[88:91], v[92:95], v[232:235]\n"
    "ds_read_b128 v[68:71], v11 offset:6144\n"
    "ds_read_b64 v[64:65], v10 offset:2048\n"
    "ds_read_b64 v[66:67], v10 offset:2056\n"
    "ds_read_b128 v[76:79], v11 offset:4096\n"
    "ds_read_b64 v[72:73], v10 offset:2560\n"
    "ds_read_b64 v[74:75], v10 offset:2568\n"
    "ds_read_b128 v[116:119], v12 offset:6144\n"
    "ds_read_b64 v[112:113], v10 offset:2112\n"
    "ds_read_b64 v[114:115], v10 offset:2120\n"
    "ds_read_b128 v[124:127], v12 offset:4096\n"
    "ds_read_b64 v[120:121], v10 offset:2624\n"
    "ds_read_b64 v[122:123], v10 offset:2632\n"
    "s_add_u32 s94, s8, 0\n"
    "s_addc_u32 s95, s9, 0\n"
    "global_load_dwordx4 v[144:147], v2, s[94:95] offset:0 sc1\n"
    "v_mfma_f32_16x16x32_bf16 v[236:239], v[32:35], v[60:63], 0\n"
    "v_mfma_f32_16x16x32_bf16 v[232:235], v[32:35], v[52:55], v[232:235]\n"
    "global_load_dwordx4 v[148:151], v2, s[94:95] offset:1024 sc1\n"
    "v_mfma_f32_16x16x32_bf16 v[236:239], v[40:43], v[52:55], v[236:239]\n"
    "v_mfma_f32_16x16x32_bf16 v[236:239], v[48:51], v[44:47], v[236:239]\n"
    "global_load_dwordx4 v[152:155], v2, s[94:95] offset:2048 sc1\n"
    "v_mfma_f32_16x16x32_bf16 v[232:235], v[48:51], v[36:39], v[232:235]\n"
    "v_mfma_f32_16x16x32_bf16 v[236:239], v[56:59], v[36:39], v[236:239]\n"
    "global_load_dwordx4 v[156:159], v2, s[94:95] offset:3072 sc1\n"
    "v_mfma_f32_16x16x32_bf16 v[236:239], v[80:83], v[108:111], v[236:239]\n"
    "v_mfma_f32_16x16x32_bf16 v[232:235], v[80:83], v[100:103], v[232:235]\n"
    "s_getreg_b32 s55, hwreg(HW_REG_IB_STS, 0, 4)\n"
    "s_or_b32 s55, s55, s75\n"
    "s_cmp_gt_u32 s55, 6\n"
    "s_cbranch_scc1 .Lk_s12\n"
    "v_lshlrev_b32 v22, 12, v1\n"
    "s_add_u32 s48, s98, 0\n"
    "s_addc_u32 s49, s99, 0\n"
    "s_waitcnt vmcnt(6)\n"
    "s_mov_b64 exec, 3\n"
    "global_store_dwordx2 v22, v[4:5], s[48:49] sc1\n"
    "s_mov_b64 exec, -1\n"
    "s_lshl2_add_u32 s58, s28, s27\n"
    "s_cmp_ge_u32 s58, 4\n"
    "s_cbranch_scc1 .Lk_s13\n"
    "s_lshl2_add_u32 s58, s26, s27\n"
    "s_lshl_b32 s58, s58, 7\n"
    "s_add_u32 s58, s58, 18155520\n"
    "s_add_u32 s60, s22, s58\n"
    "s_addc_u32 s61, s23, 0\n"
    "s_mov_b64 exec, 1\n"
    "global_store_dwordx2 v3, v[4:5], s[60:61] sc1\n"
    "s_mov_b64 exec, -1\n"
    ".Lk_s13:\n"
    "s_mov_b32 s75, 64\n"
    ".Lk_s12:\n"
    "v_mfma_f32_16x16x32_bf16 v[236:239], v[88:91], v[100:103], v[236:239]\n"
    "s_add_u32 s94, s8, 4096\n"
    "s_addc_u32 s95, s9, 0\n"
    "global_load_dwordx4 v[160:163], v2, s[94:95] offset:0 sc1\n"
    "v_mfma_f32_16x16x32_bf16 v[236:239], v[96:99], v[92:95], v[236:239]\n"
    "v_mfma_f32_16x16x32_bf16 v[232:235], v[96:99], v[84:87], v[232:235]\n"
    "s_getreg_b32 s55, hwreg(HW_REG_IB_STS, 0, 4)\n"
    "s_or_b32 s55, s55, s75\n"
    "s_cmp_gt_u32 s55, 7\n"
    "s_cbranch_scc1 .Lk_s14\n"
    "v_lshlrev_b32 v22, 12, v1\n"
    "s_add_u32 s48, s98, 0\n"
    "s_addc_u32 s49, s99, 0\n"
    "s_waitcnt vmcnt(7)\n"
    "s_mov_b64 exec, 3\n"
    "global_store_dwordx2 v22, v[4:5], s[48:49] sc1\n"
    "s_mov_b64 exec, -1\n"
    "s_lshl2_add_u32 s58, s28, s27\n"
    "s_cmp_ge_u32 s58, 4\n"
    "s_cbranch_scc1 .Lk_s15\n"
    "s_lshl2_add_u32 s58, s26, s27\n"
    "s_lshl_b32 s58, s58, 7\n"
    "s_add_u32 s58, s58, 18155520\n"
    "s_add_u32 s60, s22, s58\n"
    "s_addc_u32 s61, s23, 0\n"
    "s_mov_b64 exec, 1\n"
    "global_store_dwordx2 v3, v[4:5], s[60:61] sc1\n"
    "s_mov_b64 exec, -1\n"
    ".Lk_s15:\n"
    "s_mov_b32 s75, 64\n"
    ".Lk_s14:\n"
    "v_mfma_f32_16x16x32_bf16 v[236:239], v[104:107], v[84:87], v[236:239]\n"
    "s_nop 2\n"
    "ds_write_b128 v13, v[232:235] offset:16384\n"
    "s_nop 3\n"
    "ds_write_b128 v13, v[236:239] offset:17408\n"
    "global_load_dwordx4 v[164:167], v2, s[94:95] offset:1024 sc1\n"
    "v_mfma_f32_16x16x32_bf16 v[224:227], v[40:43], v[60:63], 0\n"
    "v_mfma_f32_16x16x32_bf16 v[228:231], v[48:51], v[60:63], 0\n"
    "s_getreg_b32 s55, hwreg(HW_REG_IB_STS, 0, 4)\n"
    "s_or_b32 s55, s55, s75\n"
    "s_cmp_gt_u32 s55, 8\n"
    "s_cbranch_scc1 .Lk_s16\n"
    "v_lshlrev_b32 v22, 12, v1\n"
    "s_add_u32 s48, s98, 0\n"
    "s_addc_u32 s49, s99, 0\n"
    "s_waitcnt vmcnt(8)\n"
    "s_mov_b64 exec, 3\n"
    "global_store_dwordx2 v22, v[4:5], s[48:49] sc1\n"
    "s_mov_b64 exec, -1\n"
    "s_lshl2_add_u32 s58, s28, s27\n"
    "s_cmp_ge_u32 s58, 4\n"
    "s_cbranch_scc1 .Lk_s17\n"
    "s_lshl2_add_u32 s58, s26, s27\n"
    "s_lshl_b32 s58, s58, 7\n"
    "s_add_u32 s58, s58, 18155520\n"
    "s_add_u32 s60, s22, s58\n"
    "s_addc_u32 s61, s23, 0\n"
    "s_mov_b64 exec, 1\n"
    "global_store_dwordx2 v3, v[4:5], s[60:61] sc1\n"
    "s_mov_b64 exec, -1\n"
    ".Lk_s17:\n"
    "s_mov_b32 s75, 64\n"
    ".Lk_s16:\n"
    "v_mfma_f32_16x16x32_bf16 v[224:227], v[48:51], v[52:55], v[224:227]\n"
    "global_load_dwordx4 v[168:171], v2, s[94:95] offset:2048 sc1\n"
    "v_mfma_f32_16x16x32_bf16 v[228:231], v[56:59], v[52:55], v[228:231]\n"
    "v_mfma_f32_16x16x32_bf16 v[224:227], v[56:59], v[44:47], v[224:227]\n"
    "s_getreg_b32 s55, hwreg(HW_REG_IB_STS, 0, 4)\n"
    "s_or_b32 s55, s55, s75\n"
    "s_cmp_gt_u32 s55, 9\n"
    "s_cbranch_scc1 .Lk_s18\n"
    "v_lshlrev_b32 v22, 12, v1\n"
    "s_add_u32 s48, s98, 0\n"
    "s_addc_u32 s49, s99, 0\n"
    "s_waitcnt vmcnt(9)\n"
    "s_mov_b64 exec, 3\n"
    "global_store_dwordx2 v22, v[4:5], s[48:49] sc1\n"
    "s_mov_b64 exec, -1\n"
    "s_lshl2_add_u32 s58, s28, s27\n"
    "s_cmp_ge_u32 s58, 4\n"
    "s_cbranch_scc1 .Lk_s19\n"
    "s_lshl2_add_u32 s58, s26, s27\n"
    "s_lshl_b32 s58, s58, 7\n"
    "s_add_u32 s58, s58, 18155520\n"
    "s_add_u32 s60, s22, s58\n"
    "s_addc_u32 s61, s23, 0\n"
    "s_mov_b64 exec, 1\n"
    "global_store_dwordx2 v3, v[4:5], s[60:61] sc1\n"
    "s_mov_b64 exec, -1\n"
    ".Lk_s19:\n"
    "s_mov_b32 s75, 64\n"
    ".Lk_s18:\n"
    "v_mfma_f32_16x16x32_bf16 v[224:227], v[88:91], v[108:111], v[224:227]\n"
    "s_waitcnt lgkmcnt(0)\n"
    "s_barrier\n"
    "ds_read_b128 v[132:135], v11 offset:2048\n"
    "ds_read_b64 v[128:129], v10 offset:3072\n"
    "ds_read_b64 v[130:131], v10 offset:3080\n"
    "ds_read_b128 v[140:143], v11 offset:0\n"
    "ds_read_b64 v[136:137], v10 offset:3584\n"
    "ds_read_b64 v[138:139], v10 offset:3592\n"
    "ds_read_b64 v[252:253], v15 offset:4096\n"
    "ds_read_b64 v[254:255], v16 offset:4096\n"
    "global_load_dwordx4 v[172:175], v2, s[94:95] offset:3072 sc1\n"
    "v_mfma_f32_16x16x32_bf16 v[228:231], v[96:99], v[108:111], v[228:231]\n"
    "v_mfma_f32_16x16x32_bf16 v[224:227], v[96:99], v[100:103], v[224:227]\n"
    "s_getreg_b32 s55, hwreg(HW_REG_IB_STS, 0, 4)\n"
    "s_or_b32 s55, s55, s75\n"
    "s_cmp_gt_u32 s55, 10\n"
    "s_cbranch_scc1 .Lk_s20\n"
    "v_lshlrev_b32 v22, 12, v1\n"
    "s_add_u32 s48, s98, 0\n"
    "s_addc_u32 s49, s99, 0\n"
    "s_waitcnt vmcnt(10)\n"
    "s_mov_b64 exec, 3\n"
    "global_store_dwordx2 v22, v[4:5], s[48:49] sc1\n"
    "s_mov_b64 exec, -1\n"
    "s_lshl2_add_u32 s58, s28, s27\n"
    "s_cmp_ge_u32 s58, 4\n"
    "s_cbranch_scc1 .Lk_s21\n"
    "s_lshl2_add_u32 s58, s26, s27\n"
    "s_lshl_b32 s58, s58, 7\n"
    "s_add_u32 s58, s58, 18155520\n"
    "s_add_u32 s60, s22, s58\n"
    "s_addc_u32 s61, s23, 0\n"
    "s_mov_b64 exec, 1\n"
    "global_store_dwordx2 v3, v[4:5], s[60:61] sc1\n"
    "s_mov_b64 exec, -1\n"
    ".Lk_s21:\n"
    "s_mov_b32 s75, 64\n"
    ".Lk_s20:\n"
    "v_mfma_f32_16x16x32_bf16 v[228:231], v[104:107], v[100:103], v[228:231]\n"
    "s_add_u32 s94, s8, 8192\n"
    "s_addc_u32 s95, s9, 0\n"
    "global_load_dwordx4 v[176:179], v2, s[94:95] offset:0 sc1\n"
    "v_mfma_f32_16x16x32_bf16 v[224:227], v[104:107], v[92:95], v[224:227]\n"
    "v_mfma_f32_16x16x32_bf16 v[228:231], v[32:35], v[76:79], v[228:231]\n"
    "s_getreg_b32 s55, hwreg(HW_REG_IB_STS, 0, 4)\n"
    "s_or_b32 s55, s55, s75\n"
    "s_cmp_gt_u32 s55, 11\n"
    "s_cbranch_scc1 .Lk_s22\n"
    "v_lshlrev_b32 v22, 12, v1\n"
    "s_add_u32 s48, s98, 0\n"
    "s_addc_u32 s49, s99, 0\n"
    "s_waitcnt vmcnt(11)\n"
    "s_mov_b64 exec, 3\n"
    "global_store_dwordx2 v22, v[4:5], s[48:49] sc1\n"
    "s_mov_b64 exec, -1\n"
    "s_lshl2_add_u32 s58, s28, s27\n"
    "s_cmp_ge_u32 s58, 4\n"
    "s_cbranch_scc1 .Lk_s23\n"
    "s_lshl2_add_u32 s58, s26, s27\n"
    "s_lshl_b32 s58, s58, 7\n"
    "s_add_u32 s58, s58, 18155520\n"
    "s_add_u32 s60, s22, s58\n"
    "s_addc_u32 s61, s23, 0\n"
    "s_mov_b64 exec, 1\n"
    "global_store_dwordx2 v3, v[4:5], s[60:61] sc1\n"
    "s_mov_b64 exec, -1\n"
    ".Lk_s23:\n"
    "s_mov_b32 s75, 64\n"
    ".Lk_s22:\n"
    "v_mfma_f32_16x16x32_bf16 v[224:227], v[32:35], v[68:71], v[224:227]\n"
    "global_load_dwordx4 v[180:183], v2, s[94:95] offset:1024 sc1\n"
    "v_mfma_f32_16x16x32_bf16 v[228:231], v[40:43], v[68:71], v[228:231]\n"
    "v_mfma_f32_16x16x32_bf16 v[228:231], v[64:67], v[44:47], v[228:231]\n"
    "s_getreg_b32 s55, hwreg(HW_REG_IB_STS, 0, 4)\n"
    "s_or_b32 s55, s55, s75\n"
    "s_cmp_gt_u32 s55, 12\n"
    "s_cbranch_scc1 .Lk_s24\n"
    "v_lshlrev_b32 v22, 12, v1\n"
    "s_add_u32 s48, s98, 0\n"
    "s_addc_u32 s49, s99, 0\n"
    "s_waitcnt vmcnt(12)\n"
    "s_mov_b64 exec, 3\n"
    "global_store_dwordx2 v22, v[4:5], s[48:49] sc1\n"
    "s_mov_b64 exec, -1\n"
    "s_lshl2_add_u32 s58, s28, s27\n"
    "s_cmp_ge_u32 s58, 4\n"
    "s_cbranch_scc1 .Lk_s25\n"
    "s_lshl2_add_u32 s58, s26, s27\n"
    "s_lshl_b32 s58, s58, 7\n"
    "s_add_u32 s58, s58, 18155520\n"
    "s_add_u32 s60, s22, s58\n"
    "s_addc_u32 s61, s23, 0\n"
    "s_mov_b64 exec, 1\n"
    "global_store_dwordx2 v3, v[4:5], s[60:61] sc1\n"
    "s_mov_b64 exec, -1\n"
    ".Lk_s25:\n"
    "s_mov_b32 s75, 64\n"
    ".Lk_s24:\n"
    "v_mfma_f32_16x16x32_bf16 v[224:227], v[64:67], v[36:39], v[224:227]\n"
    "global_load_dwordx4 v[184:187], v2, s[94:95] offset:2048 sc1\n"
    "v_mfma_f32_16x16x32_bf16 v[228:231], v[72:75], v[36:39], v[228:231]\n"
    "v_mfma_f32_16x16x32_bf16 v[228:231], v[80:83], v[124:127], v[228:231]\n"
    "s_getreg_b32 s55, hwreg(HW_REG_IB_STS, 0, 4)\n"
    "s_or_b32 s55, s55, s75\n"
    "s_cmp_gt_u32 s55, 13\n"
    "s_cbranch_scc1 .Lk_s26\n"
    "v_lshlrev_b32 v22, 12, v1\n"
    "s_add_u32 s48, s98, 0\n"
    "s_addc_u32 s49, s99, 0\n"
    "s_waitcnt vmcnt(13)\n"
    "s_mov_b64 exec, 3\n"
    "global_store_dwordx2 v22, v[4:5], s[48:49] sc1\n"
    "s_mov_b64 exec, -1\n"
    "s_lshl2_add_u32 s58, s28, s27\n"
    "s_cmp_ge_u32 s58, 4\n"
    "s_cbranch_scc1 .Lk_s27\n"
    "s_lshl2_add_u32 s58, s26, s27\n"
    "s_lshl_b32 s58, s58, 7\n"
    "s_add_u32 s58, s58, 18155520\n"
    "s_add_u32 s60, s22, s58\n"
    "s_addc_u32 s61, s23, 0\n"
    "s_mov_b64 exec, 1\n"
    "global_store_dwordx2 v3, v[4:5], s[60:61] sc1\n"
    "s_mov_b64 exec, -1\n"
    ".Lk_s27:\n"
    "s_mov_b32 s75, 64\n"
    ".Lk_s26:\n"
    "v_mfma_f32_16x16x32_bf16 v[224:227], v[80:83], v[116:119], v[224:227]\n"
    "global_load_dwordx4 v[188:191], v2, s[94:95] offset:3072 sc1\n"
    "v_mfma_f32_16x16x32_bf16 v[228:231], v[88:91], v[116:119], v[228:231]\n"
    "v_mfma_f32_16x16x32_bf16 v[228:231], v[112:115], v[92:95], v[228:231]\n"
    "s_getreg_b32 s55, hwreg(HW_REG_IB_STS, 0, 4)\n"
    "s_or_b32 s55, s55, s75\n"
    "s_cmp_gt_u32 s55, 14\n"
    "s_cbranch_scc1 .Lk_s28\n"
    "v_lshlrev_b32 v22, 12, v1\n"
    "s_add_u32 s48, s98, 0\n"
    "s_addc_u32 s49, s99, 0\n"
    "s_waitcnt vmcnt(14)\n"
    "s_mov_b64 exec, 3\n"
    "global_store_dwordx2 v22, v[4:5], s[48:49] sc1\n"
    "s_mov_b64 exec, -1\n"
    "s_lshl2_add_u32 s58, s28, s27\n"
    "s_cmp_ge_u32 s58, 4\n"
    "s_cbranch_scc1 .Lk_s29\n"
    "s_lshl2_add_u32 s58, s26, s27\n"
    "s_lshl_b32 s58, s58, 7\n"
    "s_add_u32 s58, s58, 18155520\n"
    "s_add_u32 s60, s22, s58\n"
    "s_addc_u32 s61, s23, 0\n"
    "s_mov_b64 exec, 1\n"
    "global_store_dwordx2 v3, v[4:5], s[60:61] sc1\n"
    "s_mov_b64 exec, -1\n"
    ".Lk_s29:\n"
    "s_mov_b32 s75, 64\n"
    ".Lk_s28:\n"
    "v_mfma_f32_16x16x32_bf16 v[224:227], v[112:115], v[84:87], v[224:227]\n"
    "s_add_u32 s94, s8, 12288\n"
    "s_addc_u32 s95, s9, 0\n"
    "global_load_dwordx4 v[192:195], v2, s[94:95] offset:0 sc1\n"
    "v_mfma_f32_16x16x32_bf16 v[228:231], v[120:123], v[84:87], v[228:231]\n"
    "s_nop 3\n"
    "ds_write_b128 v13, v[224:227] offset:0\n"
    "s_nop 2\n"
    "ds_write_b128 v13, v[228:231] offset:1024\n"
    "s_cmp_lg_u32 s75, 0\n"
    "s_cbranch_scc1 .Lk_s30\n"
    "v_lshlrev_b32 v22, 12, v1\n"
    "s_add_u32 s48, s98, 0\n"
    "s_addc_u32 s49, s99, 0\n"
    "s_waitcnt vmcnt(15)\n"
    "s_mov_b64 exec, 3\n"
    "global_store_dwordx2 v22, v[4:5], s[48:49] sc1\n"
    "s_mov_b64 exec, -1\n"
    "s_lshl2_add_u32 s58, s28, s27\n"
    "s_cmp_ge_u32 s58, 4\n"
    "s_cbranch_scc1 .Lk_s31\n"
    "s_lshl2_add_u32 s58, s26, s27\n"
    "s_lshl_b32 s58, s58, 7\n"
    "s_add_u32 s58, s58, 18155520\n"
    "s_add_u32 s60, s22, s58\n"
    "s_addc_u32 s61, s23, 0\n"
    "s_mov_b64 exec, 1\n"
    "global_store_dwordx2 v3, v[4:5], s[60:61] sc1\n"
    "s_mov_b64 exec, -1\n"
    ".Lk_s31:\n"
    "s_mov_b32 s75, 64\n"
    ".Lk_s30:\n"
    "v_min_u32 v22, 55, v1\n"
    "s_mul_i32 s55, s28, 56\n"
    "v_add_lshl_u32 v15, v22, s55, 4\n"
    "s_mul_i32 s57, s28, 7168\n"
    "s_add_u32 s52, s80, s57\n"
    "s_addc_u32 s53, s81, 0\n"
    "s_add_u32 s92, s52, 4096\n"
    "s_addc_u32 s93, s53, 0\n"
    "v_min_u32 v22, 27, v1\n"
    "s_mul_i32 s55, s28, 28\n"
    "v_add_lshl_u32 v7, v22, s55, 4\n"
    "global_load_dwordx4 v[196:199], v2, s[94:95] offset:1024 sc1\n"
    "s_waitcnt lgkmcnt(6)\n"
    "v_mfma_f32_16x16x32_bf16 v[236:239], v[32:35], v[140:143], 0\n"
    "v_mfma_f32_16x16x32_bf16 v[232:235], v[32:35], v[132:135], 0\n"
    "global_load_dwordx4 v[200:203], v2, s[94:95] offset:2048 sc1\n"
    "v_mfma_f32_16x16x32_bf16 v[236:239], v[40:43], v[132:135], v[236:239]\n"
    "v_mfma_f32_16x16x32_bf16 v[236:239], v[128:131], v[44:47], v[236:239]\n"
    "global_load_dwordx4 v[204:207], v2, s[94:95] offset:3072 sc1\n"
    "v_mfma_f32_16x16x32_bf16 v[232:235], v[128:131], v[36:39], v[232:235]\n"
    "s_mov_b32 s96, 0\n"
    "global_load_dwordx2 v[20:21], v15, s[78:79] sc1\n"
    "s_waitcnt lgkmcnt(4)\n"
    "v_mfma_f32_16x16x32_bf16 v[236:239], v[136:139], v[36:39], v[236:239]\n"
    "ds_read_b128 v[132:135], v12 offset:2048\n"
    "ds_read_b64 v[128:129], v10 offset:3136\n"
    "ds_read_b64 v[130:131], v10 offset:3144\n"
    "ds_read_b128 v[140:143], v12 offset:0\n"
    "ds_read_b64 v[136:137], v10 offset:3648\n"
    "ds_read_b64 v[138:139], v10 offset:3656\n"
    "s_waitcnt lgkmcnt(6)\n"
    "s_barrier\n"
    "s_waitcnt vmcnt(18)\n"
    "v_bitop3_b32 v241, v241, v243, s34 bitop3:0x7e\n"
    "s_add_u32 s94, s8, 16384\n"
    "s_addc_u32 s95, s9, 0\n"
    "global_load_dwordx4 v[208:211], v2, s[94:95] offset:0 sc1\n"
    "s_waitcnt vmcnt(18)\n"
    "v_bitop3_b32 v245, v245, v247, s34 bitop3:0x7e\n"
    "v_or_b32 v241, v241, v245\n"
    "v_cmp_ne_u32 vcc, 0, v241\n"
    "s_cbranch_vccnz .Lk_r32\n"
    ".Lk_c33:\n"
    "v_and_b32 v241, 0xffff0000, v240\n"
    "v_and_b32 v243, 0xffff0000, v242\n"
    "v_and_b32 v245, 0xffff0000, v244\n"
    "v_and_b32 v247, 0xffff0000, v246\n"
    "v_rcp_f32 v241, v241\n"
    "v_lshlrev_b32 v240, 16, v240\n"
    "v_rcp_f32 v243, v243\n"
    "global_load_dwordx4 v[212:215], v2, s[94:95] offset:1024 sc1\n"
    "v_lshlrev_b32 v242, 16, v242\n"
    "v_rcp_f32 v245, v245\n"
    "v_lshlrev_b32 v244, 16, v244\n"
    "v_rcp_f32 v247, v247\n"
    "v_lshlrev_b32 v246, 16, v246\n"
    "v_lshlrev_b32 v248, 16, v254\n"
    "v_and_b32 v249, 0xffff0000, v254\n"
    "v_lshlrev_b32 v250, 16, v255\n"
    "v_and_b32 v251, 0xffff0000, v255\n"
    "v_sub_f32 v240, v248, v240\n"
    "v_sub_f32 v242, v249, v242\n"
    "global_load_dwordx4 v[216:219], v2, s[94:95] offset:2048 sc1\n"
    "v_sub_f32 v244, v250, v244\n"
    "v_sub_f32 v246, v251, v246\n"
    "v_mul_f32 v240, v240, v241\n"
    "v_mul_f32 v241, v242, v243\n"
    "v_mul_f32 v242, v244, v245\n"
    "v_mul_f32 v243, v246, v247\n"
    "v_and_b32 v244, 0xffff0000, v253\n"
    "v_lshlrev_b32 v245, 16, v253\n"
    "v_and_b32 v246, 0xffff0000, v252\n"
    "v_lshlrev_b32 v247, 16, v252\n"
    "v_mul_f32 v244, v244, v19\n"
    "global_load_dwordx4 v[220:223], v2, s[94:95] offset:3072 sc1\n"
    "v_mul_f32 v245, v245, v19\n"
    "v_mul_f32 v246, v246, v19\n"
    "v_mul_f32 v247, v247, v19\n"
    "s_lshl_b32 s55, s29, 7\n"
    "s_add_u32 s55, s55, 4636672\n"
    "s_add_u32 s46, s22, s55\n"
    "s_addc_u32 s47, s23, 0\n"
    "ds_read_b128 v[252:255], v14 offset:0\n"
    "ds_read_b128 v[224:227], v14 offset:2048\n"
    "ds_read_b128 v[228:231], v14 offset:4096\n"
    "ds_read_b128 v[28:31], v14 offset:6144\n"
    "v_mfma_f32_16x16x32_bf16 v[232:235], v[40:43], v[76:79], v[232:235]\n"
    "v_mfma_f32_16x16x32_bf16 v[236:239], v[48:51], v[76:79], v[236:239]\n"
    "v_mfma_f32_16x16x32_bf16 v[232:235], v[48:51], v[68:71], v[232:235]\n"
    "v_mfma_f32_16x16x32_bf16 v[236:239], v[56:59], v[68:71], v[236:239]\n"
    "s_waitcnt lgkmcnt(3)\n"
    "v_pk_add_f32 v[252:253], v[252:253], v[244:245]\n"
    "v_pk_add_f32 v[254:255], v[254:255], v[246:247]\n"
    "s_waitcnt lgkmcnt(2)\n"
    "v_pk_add_f32 v[252:253], v[252:253], v[224:225]\n"
    "v_pk_add_f32 v[254:255], v[254:255], v[226:227]\n"
    "s_waitcnt lgkmcnt(1)\n"
    "v_pk_add_f32 v[252:253], v[252:253], v[228:229]\n"
    "v_pk_add_f32 v[254:255], v[254:255], v[230:231]\n"
    "s_waitcnt lgkmcnt(0)\n"
    "v_pk_add_f32 v[252:253], v[252:253], v[28:29]\n"
    "v_pk_add_f32 v[254:255], v[254:255], v[30:31]\n"
    "v_pk_fma_f32 v[248:249], v[240:241], v[252:253], v[248:249]\n"
    "v_pk_fma_f32 v[250:251], v[242:243], v[254:255], v[250:251]\n"
    "v_cvt_pk_bf16_f32 v24, v248, v249\n"
    "v_cvt_pk_bf16_f32 v25, v250, v251\n"
    "global_store_dwordx2 v17, v[24:25], s[46:47] sc1\n"
    "s_mov_b32 s75, 0\n"
    "s_getreg_b32 s55, hwreg(HW_REG_IB_STS, 0, 4)\n"
    "s_lshl4_add_u32 s55, s96, s55\n"
    "s_cmp_gt_u32 s55, 5\n"
    "s_cbranch_scc1 .Lk_s35\n"
    "s_waitcnt vmcnt(5)\n"
    "v_cmp_ne_u64 vcc, s[34:35], v[20:21]\n"
    "s_mov_b32 s96, 2\n"
    "s_cbranch_vccnz .Lk_s36\n"
    "s_mov_b32 m0, s57\n"
    "s_nop 0\n"
    "global_load_lds_dwordx4 v2, s[52:53] offset:0 sc1\n"
    "global_load_lds_dwordx4 v2, s[52:53] offset:1024 sc1\n"
    "global_load_lds_dwordx4 v2, s[52:53] offset:2048 sc1\n"
    "global_load_lds_dwordx4 v2, s[52:53] offset:3072 sc1\n"
    "s_add_u32 m0, s57, 4096\n"
    "s_nop 0\n"
    "global_load_lds_dwordx4 v2, s[92:93] offset:0 sc1\n"
    "global_load_lds_dwordx4 v2, s[92:93] offset:1024 sc1\n"
    "global_load_lds_dwordx4 v2, s[92:93] offset:2048 sc1\n"
    "s_mov_b32 s96, 1\n"
    ".Lk_s36:\n"
    ".Lk_s35:\n"
    "v_mfma_f32_16x16x32_bf16 v[232:235], v[56:59], v[60:63], v[232:235]\n"
    "v_mfma_f32_16x16x32_bf16 v[236:239], v[64:67], v[60:63], v[236:239]\n"
    "v_mfma_f32_16x16x32_bf16 v[232:235], v[64:67], v[52:55], v[232:235]\n"
    "s_getreg_b32 s55, hwreg(HW_REG_IB_STS, 0, 4)\n"
    "s_lshl4_add_u32 s55, s96, s55\n"
    "s_cmp_gt_u32 s55, 5\n"
    "s_cbranch_scc1 .Lk_s37\n"
    "s_waitcnt vmcnt(5)\n"
    "v_cmp_ne_u64 vcc, s[34:35], v[20:21]\n"
    "s_mov_b32 s96, 2\n"
    "s_cbranch_vccnz .Lk_s38\n"
    "s_mov_b32 m0, s57\n"
    "s_nop 0\n"
    "global_load_lds_dwordx4 v2, s[52:53] offset:0 sc1\n"
    "global_load_lds_dwordx4 v2, s[52:53] offset:1024 sc1\n"
    "global_load_lds_dwordx4 v2, s[52:53] offset:2048 sc1\n"
    "global_load_lds_dwordx4 v2, s[52:53] offset:3072 sc1\n"
    "s_add_u32 m0, s57, 4096\n"
    "s_nop 0\n"
    "global_load_lds_dwordx4 v2, s[92:93] offset:0 sc1\n"
    "global_load_lds_dwordx4 v2, s[92:93] offset:1024 sc1\n"
    "global_load_lds_dwordx4 v2, s[92:93] offset:2048 sc1\n"
    "s_mov_b32 s96, 1\n"
    ".Lk_s38:\n"
    ".Lk_s37:\n"
    "v_mfma_f32_16x16x32_bf16 v[236:239], v[72:75], v[52:55], v[236:239]\n"
    "v_mfma_f32_16x16x32_bf16 v[232:235], v[72:75], v[44:47], v[232:235]\n"
    "v_mfma_f32_16x16x32_bf16 v[232:235], v[88:91], v[124:127], v[232:235]\n"
    "s_getreg_b32 s55, hwreg(HW_REG_IB_STS, 0, 4)\n"
    "s_lshl4_add_u32 s55, s96, s55\n"
    "s_cmp_gt_u32 s55, 5\n"
    "s_cbranch_scc1 .Lk_s39\n"
    "s_waitcnt vmcnt(5)\n"
    "v_cmp_ne_u64 vcc, s[34:35], v[20:21]\n"
    "s_mov_b32 s96, 2\n"
    "s_cbranch_vccnz .Lk_s40\n"
    "s_mov_b32 m0, s57\n"
    "s_nop 0\n"
    "global_load_lds_dwordx4 v2, s[52:53] offset:0 sc1\n"
    "global_load_lds_dwordx4 v2, s[52:53] offset:1024 sc1\n"
    "global_load_lds_dwordx4 v2, s[52:53] offset:2048 sc1\n"
    "global_load_lds_dwordx4 v2, s[52:53] offset:3072 sc1\n"
    "s_add_u32 m0, s57, 4096\n"
    "s_nop 0\n"
    "global_load_lds_dwordx4 v2, s[92:93] offset:0 sc1\n"
    "global_load_lds_dwordx4 v2, s[92:93] offset:1024 sc1\n"
    "global_load_lds_dwordx4 v2, s[92:93] offset:2048 sc1\n"
    "s_mov_b32 s96, 1\n"
    ".Lk_s40:\n"
    ".Lk_s39:\n"
    "v_mfma_f32_16x16x32_bf16 v[236:239], v[96:99], v[124:127], v[236:239]\n"
    "v_mfma_f32_16x16x32_bf16 v[232:235], v[96:99], v[116:119], v[232:235]\n"
    "v_mfma_f32_16x16x32_bf16 v[236:239], v[104:107], v[116:119], v[236:239]\n"
    "s_getreg_b32 s55, hwreg(HW_REG_IB_STS, 0, 4)\n"
    "s_lshl4_add_u32 s55, s96, s55\n"
    "s_cmp_gt_u32 s55, 5\n"
    "s_cbranch_scc1 .Lk_s41\n"
    "s_waitcnt vmcnt(5)\n"
    "v_cmp_ne_u64 vcc, s[34:35], v[20:21]\n"
    "s_mov_b32 s96, 2\n"
    "s_cbranch_vccnz .Lk_s42\n"
    "s_mov_b32 m0, s57\n"
    "s_nop 0\n"
    "global_load_lds_dwordx4 v2, s[52:53] offset:0 sc1\n"
    "global_load_lds_dwordx4 v2, s[52:53] offset:1024 sc1\n"
    "global_load_lds_dwordx4 v2, s[52:53] offset:2048 sc1\n"
    "global_load_lds_dwordx4 v2, s[52:53] offset:3072 sc1\n"
    "s_add_u32 m0, s57, 4096\n"
    "s_nop 0\n"
    "global_load_lds_dwordx4 v2, s[92:93] offset:0 sc1\n"
    "global_load_lds_dwordx4 v2, s[92:93] offset:1024 sc1\n"
    "global_load_lds_dwordx4 v2, s[92:93] offset:2048 sc1\n"
    "s_mov_b32 s96, 1\n"
    ".Lk_s42:\n"
    ".Lk_s41:\n"
    "v_mfma_f32_16x16x32_bf16 v[232:235], v[104:107], v[108:111], v[232:235]\n"
    "v_mfma_f32_16x16x32_bf16 v[236:239], v[112:115], v[108:111], v[236:239]\n"
    "v_mfma_f32_16x16x32_bf16 v[232:235], v[112:115], v[100:103], v[232:235]\n"
    "s_getreg_b32 s55, hwreg(HW_REG_IB_STS, 0, 4)\n"
    "s_lshl4_add_u32 s55, s96, s55\n"
    "s_cmp_gt_u32 s55, 5\n"
    "s_cbranch_scc1 .Lk_s43\n"
    "s_waitcnt vmcnt(5)\n"
    "v_cmp_ne_u64 vcc, s[34:35], v[20:21]\n"
    "s_mov_b32 s96, 2\n"
    "s_cbranch_vccnz .Lk_s44\n"
    "s_mov_b32 m0, s57\n"
    "s_nop 0\n"
    "global_load_lds_dwordx4 v2, s[52:53] offset:0 sc1\n"
    "global_load_lds_dwordx4 v2, s[52:53] offset:1024 sc1\n"
    "global_load_lds_dwordx4 v2, s[52:53] offset:2048 sc1\n"
    "global_load_lds_dwordx4 v2, s[52:53] offset:3072 sc1\n"
    "s_add_u32 m0, s57, 4096\n"
    "s_nop 0\n"
    "global_load_lds_dwordx4 v2, s[92:93] offset:0 sc1\n"
    "global_load_lds_dwordx4 v2, s[92:93] offset:1024 sc1\n"
    "global_load_lds_dwordx4 v2, s[92:93] offset:2048 sc1\n"
    "s_mov_b32 s96, 1\n"
    ".Lk_s44:\n"
    ".Lk_s43:\n"
    "s_getreg_b32 s55, hwreg(HW_REG_IB_STS, 0, 4)\n"
    "s_or_b32 s55, s55, s75\n"
    "s_cmp_gt_u32 s55, 0\n"
    "s_cbranch_scc1 .Lk_s45\n"
    "v_lshlrev_b32 v22, 12, v1\n"
    "s_add_u32 s48, s98, 65536\n"
    "s_addc_u32 s49, s99, 0\n"
    "s_waitcnt vmcnt(0)\n"
    "s_mov_b64 exec, 3\n"
    "global_store_dwordx2 v22, v[4:5], s[48:49] sc1\n"
    "s_mov_b64 exec, -1\n"
    "s_lshl2_add_u32 s58, s28, s27\n"
    "s_cmp_ge_u32 s58, 4\n"
    "s_cbranch_scc1 .Lk_s46\n"
    "s_lshl2_add_u32 s58, s26, s27\n"
    "s_lshl_b32 s58, s58, 7\n"
    "s_add_u32 s58, s58, 18159616\n"
    "s_add_u32 s60, s22, s58\n"
    "s_addc_u32 s61, s23, 0\n"
    "s_mov_b64 exec, 1\n"
    "global_store_dwordx2 v3, v[4:5], s[60:61] sc1\n"
    "s_mov_b64 exec, -1\n"
    ".Lk_s46:\n"
    "s_mov_b32 s75, 64\n"
    ".Lk_s45:\n"
    "v_mfma_f32_16x16x32_bf16 v[236:239], v[120:123], v[100:103], v[236:239]\n"
    "v_min_u32 v6, 6, v1\n"
    "v_lshlrev_b32 v9, 4, v6\n"
    "s_add_u32 s60, s22, s97\n"
    "s_addc_u32 s61, s23, 0\n"
    "global_load_dwordx2 v[24:25], v9, s[60:61] sc1\n"
    "v_mfma_f32_16x16x32_bf16 v[232:235], v[120:123], v[92:95], v[232:235]\n"
    "v_mfma_f32_16x16x32_bf16 v[236:239], v[80:83], v[140:143], v[236:239]\n"
    "s_getreg_b32 s55, hwreg(HW_REG_IB_STS, 0, 4)\n"
    "s_lshl4_add_u32 s55, s96, s55\n"
    "s_cmp_gt_u32 s55, 6\n"
    "s_cbranch_scc1 .Lk_s47\n"
    "s_waitcnt vmcnt(6)\n"
    "v_cmp_ne_u64 vcc, s[34:35], v[20:21]\n"
    "s_mov_b32 s96, 2\n"
    "s_cbranch_vccnz .Lk_s48\n"
    "s_mov_b32 m0, s57\n"
    "s_nop 0\n"
    "global_load_lds_dwordx4 v2, s[52:53] offset:0 sc1\n"
    "global_load_lds_dwordx4 v2, s[52:53] offset:1024 sc1\n"
    "global_load_lds_dwordx4 v2, s[52:53] offset:2048 sc1\n"
    "global_load_lds_dwordx4 v2, s[52:53] offset:3072 sc1\n"
    "s_add_u32 m0, s57, 4096\n"
    "s_nop 0\n"
    "global_load_lds_dwordx4 v2, s[92:93] offset:0 sc1\n"
    "global_load_lds_dwordx4 v2, s[92:93] offset:1024 sc1\n"
    "global_load_lds_dwordx4 v2, s[92:93] offset:2048 sc1\n"
    "s_mov_b32 s96, 1\n"
    ".Lk_s48:\n"
    ".Lk_s47:\n"
    "v_mfma_f32_16x16x32_bf16 v[232:235], v[80:83], v[132:135], v[232:235]\n"
    "s_getreg_b32 s55, hwreg(HW_REG_IB_STS, 0, 4)\n"
    "s_or_b32 s55, s55, s75\n"
    "s_cmp_gt_u32 s55, 1\n"
    "s_cbranch_scc1 .Lk_s49\n"
    "v_lshlrev_b32 v22, 12, v1\n"
    "s_add_u32 s48, s98, 65536\n"
    "s_addc_u32 s49, s99, 0\n"
    "s_waitcnt vmcnt(1)\n"
    "s_mov_b64 exec, 3\n"
    "global_store_dwordx2 v22, v[4:5], s[48:49] sc1\n"
    "s_mov_b64 exec, -1\n"
    "s_lshl2_add_u32 s58, s28, s27\n"
    "s_cmp_ge_u32 s58, 4\n"
    "s_cbranch_scc1 .Lk_s50\n"
    "s_lshl2_add_u32 s58, s26, s27\n"
    "s_lshl_b32 s58, s58, 7\n"
    "s_add_u32 s58, s58, 18159616\n"
    "s_add_u32 s60, s22, s58\n"
    "s_addc_u32 s61, s23, 0\n"
    "s_mov_b64 exec, 1\n"
    "global_store_dwordx2 v3, v[4:5], s[60:61] sc1\n"
    "s_mov_b64 exec, -1\n"
    ".Lk_s50:\n"
    "s_mov_b32 s75, 64\n"
    ".Lk_s49:\n"
    "v_mfma_f32_16x16x32_bf16 v[236:239], v[88:91], v[132:135], v[236:239]\n"
    "v_mfma_f32_16x16x32_bf16 v[236:239], v[128:131], v[92:95], v[236:239]\n"
    "s_getreg_b32 s55, hwreg(HW_REG_IB_STS, 0, 4)\n"
    "s_lshl4_add_u32 s55, s96, s55\n"
    "s_cmp_gt_u32 s55, 6\n"
    "s_cbranch_scc1 .Lk_s51\n"
    "s_waitcnt vmcnt(6)\n"
    "v_cmp_ne_u64 vcc, s[34:35], v[20:21]\n"
    "s_mov_b32 s96, 2\n"
    "s_cbranch_vccnz .Lk_s52\n"
    "s_mov_b32 m0, s57\n"
    "s_nop 0\n"
    "global_load_lds_dwordx4 v2, s[52:53] offset:0 sc1\n"
    "global_load_lds_dwordx4 v2, s[52:53] offset:1024 sc1\n"
    "global_load_lds_dwordx4 v2, s[52:53] offset:2048 sc1\n"
    "global_load_lds_dwordx4 v2, s[52:53] offset:3072 sc1\n"
    "s_add_u32 m0, s57, 4096\n"
    "s_nop 0\n"
    "global_load_lds_dwordx4 v2, s[92:93] offset:0 sc1\n"
    "global_load_lds_dwordx4 v2, s[92:93] offset:1024 sc1\n"
    "global_load_lds_dwordx4 v2, s[92:93] offset:2048 sc1\n"
    "s_mov_b32 s96, 1\n"
    ".Lk_s52:\n"
    ".Lk_s51:\n"
    "v_mfma_f32_16x16x32_bf16 v[232:235], v[128:131], v[84:87], v[232:235]\n"
    "s_add_u32 s60, s78, 32768\n"
    "s_addc_u32 s61, s79, 0\n"
    "global_load_dwordx2 v[26:27], v7, s[60:61] sc1\n"
    "v_mfma_f32_16x16x32_bf16 v[236:239], v[136:139], v[84:87], v[236:239]\n"
    "s_getreg_b32 s55, hwreg(HW_REG_IB_STS, 0, 4)\n"
    "s_or_b32 s55, s55, s75\n"
    "s_cmp_gt_u32 s55, 2\n"
    "s_cbranch_scc1 .Lk_s53\n"
    "v_lshlrev_b32 v22, 12, v1\n"
    "s_add_u32 s48, s98, 65536\n"
    "s_addc_u32 s49, s99, 0\n"
    "s_waitcnt vmcnt(2)\n"
    "s_mov_b64 exec, 3\n"
    "global_store_dwordx2 v22, v[4:5], s[48:49] sc1\n"
    "s_mov_b64 exec, -1\n"
    "s_lshl2_add_u32 s58, s28, s27\n"
    "s_cmp_ge_u32 s58, 4\n"
    "s_cbranch_scc1 .Lk_s54\n"
    "s_lshl2_add_u32 s58, s26, s27\n"
    "s_lshl_b32 s58, s58, 7\n"
    "s_add_u32 s58, s58, 18159616\n"
    "s_add_u32 s60, s22, s58\n"
    "s_addc_u32 s61, s23, 0\n"
    "s_mov_b64 exec, 1\n"
    "global_store_dwordx2 v3, v[4:5], s[60:61] sc1\n"
    "s_mov_b64 exec, -1\n"
    ".Lk_s54:\n"
    "s_mov_b32 s75, 64\n"
    ".Lk_s53:\n"
    "ds_write_b128 v13, v[232:235] offset:16384\n"
    "s_nop 2\n"
    "ds_write_b128 v13, v[236:239] offset:17408\n"
    "s_cmp_lg_u32 s96, 2\n"
    "s_cbranch_scc1 .Lk_s56\n"
    "s_waitcnt vmcnt(7)\n"
    "global_load_dwordx2 v[20:21], v15, s[78:79] sc1\n"
    ".Lk_s56:\n"
    "s_waitcnt lgkmcnt(0)\n"
    "s_barrier\n"
    "s_cmp_eq_u32 s96, 1\n"
    "s_cbranch_scc1 .Lk_s55\n"
    "s_waitcnt vmcnt(0)\n"
    "v_cmp_ne_u64 vcc, s[34:35], v[20:21]\n"
    "s_cbranch_vccnz .Lk_r57\n"
    ".Lk_c58:\n"
    "s_mov_b32 m0, s57\n"
    "s_nop 0\n"
    "global_load_lds_dwordx4 v2, s[52:53] offset:0 sc1\n"
    "global_load_lds_dwordx4 v2, s[52:53] offset:1024 sc1\n"
    "global_load_lds_dwordx4 v2, s[52:53] offset:2048 sc1\n"
    "global_load_lds_dwordx4 v2, s[52:53] offset:3072 sc1\n"
    "s_add_u32 m0, s57, 4096\n"
    "s_nop 0\n"
    "global_load_lds_dwordx4 v2, s[92:93] offset:0 sc1\n"
    "global_load_lds_dwordx4 v2, s[92:93] offset:1024 sc1\n"
    "global_load_lds_dwordx4 v2, s[92:93] offset:2048 sc1\n"
    ".Lk_s55:\n"
    "v_bfe_u32 v10, v1, 2, 2\n"
    "v_lshrrev_b32 v11, 5, v1\n"
    "v_lshl_add_u32 v10, v11, 3, v10\n"
    "v_lshlrev_b32 v10, 6, v10\n"
    "v_bfe_u32 v12, v1, 4, 1\n"
    "v_lshl_or_b32 v10, v12, 5, v10\n"
    "v_and_b32 v13, 3, v1\n"
    "v_lshl_or_b32 v6, v13, 3, v10\n"
    "v_add_u32 v19, 98304, v6\n"
    "s_add_u32 s80, s80, s74\n"
    "s_addc_u32 s81, s81, 0\n"
    "s_add_u32 s94, s8, 20480\n"
    "s_addc_u32 s95, s9, 0\n"
    "global_load_dwordx4 v[224:227], v2, s[94:95] offset:0 sc1\n"
    "global_load_dwordx4 v[228:231], v2, s[94:95] offset:1024 sc1\n"
    "global_load_dwordx4 v[232:235], v2, s[94:95] offset:2048 sc1\n"
    "global_load_dwordx4 v[236:239], v2, s[94:95] offset:3072 sc1\n"
    "s_waitcnt vmcnt(4)\n"
    "s_add_u32 s60, s22, s97\n"
    "s_addc_u32 s61, s23, 0\n"
    "v_cmp_ne_u64 vcc, s[34:35], v[24:25]\n"
    "s_cbranch_vccnz .Lk_r60\n"
    ".Lk_c61:\n"
    "s_add_u32 s60, s78, 32768\n"
    "s_addc_u32 s61, s79, 0\n"
    "v_cmp_ne_u64 vcc, s[34:35], v[26:27]\n"
    "s_cbranch_vccnz .Lk_r63\n"
    ".Lk_c64:\n"
    "s_branch .Lk_G1\n"
    ".Lk_B0:\n"
    "s_mul_i32 s55, s91, 1792\n"
    "s_add_u32 s55, s55, s54\n"
    "s_add_u32 s46, s4, s55\n"
    "s_addc_u32 s47, s5, 0\n"
    "s_add_u32 s52, s22, 18163712\n"
    "s_addc_u32 s53, s23, 0\n"
    "v_cmp_gt_u32 s[62:63], 56, v1\n"
    "v_min_u32 v6, 55, v1\n"
    "v_lshlrev_b32 v6, 4, v6\n"
    "global_load_dwordx4 v[32:35], v6, s[46:47] offset:0 nt\n"
    "global_load_dwordx4 v[36:39], v6, s[46:47] offset:896 nt\n"
    "global_load_dwordx2 v[64:65], v3, s[52:53] sc1\n"
    "s_cmp_ge_u32 s27, 28\n"
    "s_cbranch_scc1 .Lk_Bh\n"
    "s_lshl_b32 s55, s29, 13\n"
    "s_add_u32 s48, s12, s55\n"
    "s_addc_u32 s49, s13, 0\n"
    "s_lshr_b32 s58, s91, 1\n"
    "s_lshl_b32 s55, s58, 12\n"
    "s_add_u32 s52, s48, s55\n"
    "s_addc_u32 s53, s49, 0\n"
    "s_and_b32 s55, s91, 1\n"
    "s_lshl_b32 s55, s55, 11\n"
    "v_add_u32 v136, s55, v2\n"
    "v_add_u32 v137, 1040, v136\n"
    "v_min_u32 v137, 4080, v137\n"
    "global_load_dwordx4 v[96:99], v136, s[52:53] offset:0 nt\n"
    "global_load_dwordx4 v[104:107], v136, s[52:53] offset:1024 nt\n"
    "global_load_dwordx2 v[100:101], v136, s[52:53] offset:16 nt\n"
    "global_load_dwordx2 v[108:109], v137, s[52:53] offset:0 nt\n"
    "s_load_dwordx2 s[92:93], s[48:49], 0x0\n"
    "s_load_dwordx2 s[94:95], s[48:49], 0x1000\n"
    "s_mul_i32 s58, s58, 4128\n"
    "s_add_u32 s58, s58, 98592\n"
    "v_add_u32 v138, s58, v136\n"
    "s_and_b32 s55, s26, 3\n"
    "s_and_b32 s56, s27, 3\n"
    "s_lshl2_add_u32 s30, s55, s56\n"
    "s_lshr_b32 s41, s26, 2\n"
    "s_mul_i32 s31, s41, 7\n"
    "s_lshr_b32 s56, s27, 2\n"
    "s_add_u32 s31, s31, s56\n"
    "s_lshl_b32 s55, s55, 1\n"
    "s_add_u32 s55, s55, s41\n"
    "s_lshl_b32 s55, s55, 12\n"
    "s_add_u32 s55, s55, 13025280\n"
    "s_add_u32 s78, s22, s55\n"
    "s_addc_u32 s79, s23, 0\n"
    "s_mul_i32 s55, s30, 28672\n"
    "s_add_u32 s55, s55, 3719168\n"
    "s_add_u32 s80, s22, s55\n"
    "s_addc_u32 s81, s23, 0\n"
    "s_lshl_b32 s55, s41, 14\n"
    "s_lshl_b32 s56, s30, 8\n"
    "s_add_u32 s55, s55, s56\n"
    "s_add_u32 s55, s55, 3686400\n"
    "s_add_u32 s82, s22, s55\n"
    "s_addc_u32 s83, s23, 0\n"
    "s_memrealtime s[60:61]\n"
    "s_min_u32 s55, s29, 223\n"
    "s_lshl_b32 s55, s55, 2\n"
    "s_load_dword s36, s[14:15], s55\n"
    "s_min_u32 s55, s29, 195\n"
    "s_mul_i32 s10, s55, 9363\n"
    "s_lshr_b32 s10, s10, 16\n"
    "s_mul_i32 s56, s10, 7\n"
    "s_sub_u32 s55, s55, s56\n"
    "s_lshl2_add_u32 s11, s55, s91\n"
    "s_cmp_lt_u32 s29, 196\n"
    "s_cselect_b32 s96, 0, 56\n"
    "s_mul_i32 s56, s10, 57344\n"
    "s_lshl_b32 s55, s11, 7\n"
    "s_add_u32 s56, s56, s55\n"
    "s_add_u32 s46, s16, s56\n"
    "s_addc_u32 s47, s17, 0\n"
    "s_add_u32 s50, s18, s56\n"
    "s_addc_u32 s51, s19, 0\n"
    "v_lshrrev_b32 v140, 3, v1\n"
    "v_mul_u32_u24 v140, 3584, v140\n"
    "v_and_b32 v141, 7, v1\n"
    "v_lshl_add_u32 v140, v141, 4, v140\n"
    "v_add_u32 v141, 28672, v140\n"
    "s_lshl_b32 s59, s91, 12\n"
    "s_add_u32 s59, s59, 65792\n"
    "v_bfe_u32 v142, v1, 2, 2\n"
    "v_lshrrev_b32 v143, 5, v1\n"
    "v_lshl_add_u32 v142, v143, 3, v142\n"
    "v_lshlrev_b32 v142, 7, v142\n"
    "v_bfe_u32 v143, v1, 4, 1\n"
    "v_lshl_or_b32 v142, v143, 11, v142\n"
    "v_and_b32 v143, 3, v1\n"
    "v_lshl_or_b32 v142, v143, 3, v142\n"
    "v_add_u32 v143, s59, v142\n"
    "s_add_u32 m0, s59, 0\n"
    "s_nop 0\n"
    "global_load_lds_dwordx4 v140, s[46:47] offset:0 nt\n"
    "s_add_u32 m0, s59, 1024\n"
    "s_nop 0\n"
    "global_load_lds_dwordx4 v141, s[46:47] offset:0 nt\n"
    "s_add_u32 m0, s59, 2048\n"
    "s_nop 0\n"
    "global_load_lds_dwordx4 v140, s[50:51] offset:0 nt\n"
    "s_add_u32 m0, s59, 3072\n"
    "s_nop 0\n"
    "global_load_lds_dwordx4 v141, s[50:51] offset:0 nt\n"
    "v_min_u32 v8, 27, v1\n"
    "v_lshlrev_b32 v8, 4, v8\n"
    "v_and_b32 v6, 15, v1\n"
    "v_lshrrev_b32 v7, 4, v1\n"
    "v_add_u32 v22, 1, v6\n"
    "v_and_b32 v10, 3, v22\n"
    "v_mul_u32_u24 v10, 8256, v10\n"
    "v_and_b32 v22, 28, v22\n"
    "v_lshl_add_u32 v22, v7, 3, v22\n"
    "v_lshl_add_u32 v10, v22, 1, v10\n"
    "s_lshl_b32 s55, s91, 7\n"
    "s_add_u32 s55, s55, 102688\n"
    "v_add_u32 v10, s55, v10\n"
    "s_lshl_b32 s55, s91, 3\n"
    "s_add_u32 s55, s55, 30\n"
    "v_lshlrev_b32 v22, 1, v6\n"
    "v_sub_u32 v22, v7, v22\n"
    "v_add_u32 v22, s55, v22\n"
    "v_lshrrev_b32 v11, 2, v22\n"
    "v_add_u32 v11, 1, v11\n"
    "v_and_b32 v11, 3, v11\n"
    "v_lshlrev_b32 v11, 4, v11\n"
    "v_lshl_or_b32 v11, v22, 6, v11\n"
    "v_add_u32 v11, 131584, v11\n"
    "v_add_u32 v22, 4, v22\n"
    "v_lshrrev_b32 v12, 2, v22\n"
    "v_add_u32 v12, 1, v12\n"
    "v_and_b32 v12, 3, v12\n"
    "v_lshlrev_b32 v12, 4, v12\n"
    "v_lshl_or_b32 v12, v22, 6, v12\n"
    "v_add_u32 v12, 131584, v12\n"
    "v_lshl_add_u32 v22, v7, 2, v6\n"
    "v_and_b32 v22, 15, v22\n"
    "v_lshlrev_b32 v22, 4, v22\n"
    "v_lshl_or_b32 v22, v7, 8, v22\n"
    "s_lshl_b32 s55, s28, 11\n"
    "s_add_u32 s55, s55, 65792\n"
    "v_add_u32 v13, s55, v22\n"
    "v_and_b32 v6, 3, v1\n"
    "v_bfe_u32 v7, v1, 3, 1\n"
    "v_bfe_u32 v22, v1, 2, 1\n"
    "v_lshrrev_b32 v16, 4, v1\n"
    "v_lshl_or_b32 v22, v16, 1, v22\n"
    "s_and_b32 s55, s91, 1\n"
    "s_lshl_b32 s55, s55, 3\n"
    "v_or_b32 v22, s55, v22\n"
    "s_lshr_b32 s56, s91, 1\n"
    "v_lshl_add_u32 v14, v6, 2, v22\n"
    "v_and_b32 v14, 15, v14\n"
    "v_lshlrev_b32 v14, 4, v14\n"
    "v_lshl_or_b32 v14, v6, 8, v14\n"
    "v_lshl_or_b32 v14, v7, 13, v14\n"
    "s_lshl_b32 s57, s56, 10\n"
    "s_add_u32 s57, s57, 65792\n"
    "v_add_u32 v14, s57, v14\n"
    "v_lshlrev_b32 v18, 4, v22\n"
    "v_lshl_or_b32 v18, v6, 2, v18\n"
    "s_lshl_b32 s57, s56, 8\n"
    "v_or_b32 v18, s57, v18\n"
    "v_lshrrev_b32 v17, 5, v18\n"
    "v_mul_u32_u24 v17, 28672, v17\n"
    "v_and_b32 v15, 31, v18\n"
    "v_lshl_add_u32 v17, v15, 1, v17\n"
    "v_lshl_add_u32 v17, v7, 6, v17\n"
    "v_lshrrev_b32 v22, 3, v18\n"
    "v_sub_u32 v22, 63, v22\n"
    "v_lshlrev_b32 v18, 3, v18\n"
    "v_lshrrev_b32 v15, 2, v22\n"
    "v_add_u32 v15, v15, v7\n"
    "v_and_b32 v15, 3, v15\n"
    "v_lshlrev_b32 v15, 4, v15\n"
    "v_lshl_or_b32 v15, v22, 6, v15\n"
    "v_and_b32 v6, 1, v6\n"
    "v_lshl_or_b32 v15, v6, 3, v15\n"
    "v_xor_b32 v16, 32, v15\n"
    "v_xor_b32 v15, 8, v15\n"
    "v_add_u32 v16, 131584, v16\n"
    "v_add_u32 v15, 131584, v15\n"
    "s_waitcnt lgkmcnt(0)\n"
    "s_mov_b32 s54, s60\n"
    "s_lshl_b32 s55, s91, 9\n"
    "s_add_u32 s55, s55, s29\n"
    "s_lshl_b32 s55, s55, 4\n"
    "s_add_u32 s55, s55, 13025280\n"
    "s_add_u32 s98, s22, s55\n"
    "s_addc_u32 s99, s23, 0\n"
    "s_lshl3_add_u32 s40, s31, s28\n"
    "s_mul_i32 s55, s30, 224\n"
    "s_add_u32 s55, s55, s40\n"
    "s_lshl_b32 s55, s55, 9\n"
    "s_add_u32 s55, s55, 5554176\n"
    "s_add_u32 s84, s22, s55\n"
    "s_addc_u32 s85, s23, 0\n"
    "s_lshl_b32 s55, s30, 7\n"
    "s_add_u32 s55, s55, s40\n"
    "s_lshl_b32 s55, s55, 4\n"
    "s_add_u32 s55, s55, 13156352\n"
    "s_add_u32 s86, s22, s55\n"
    "s_addc_u32 s87, s23, 0\n"
    "s_add_u32 s56, s26, 1\n"
    "s_and_b32 s56, s56, 3\n"
    "s_lshl1_add_u32 s56, s56, s41\n"
    "s_cmp_ge_u32 s27, 14\n"
    "s_cselect_b32 s55, 1, 0\n"
    "s_lshl1_add_u32 s37, s56, s55\n"
    "s_mul_i32 s55, s55, 14\n"
    "s_sub_u32 s55, s27, s55\n"
    "s_cmp_ge_u32 s55, 7\n"
    "s_cselect_b32 s38, 1, 0\n"
    "s_mul_i32 s56, s38, 7\n"
    "s_sub_u32 s39, s55, s56\n"
    "s_lshl3_add_u32 s55, s39, s28\n"
    "s_mul_i32 s56, s55, 28672\n"
    "s_add_u32 s56, s56, 16498688\n"
    "s_add_u32 s88, s22, s56\n"
    "s_addc_u32 s89, s23, 0\n"
    "s_lshl_b32 s55, s55, 7\n"
    "s_add_u32 s97, s55, 18118656\n"
    "s_mul_i32 s55, s40, 28672\n"
    "s_add_u32 s55, s55, 13287424\n"
    "s_add_u32 s8, s22, s55\n"
    "s_addc_u32 s9, s23, 0\n"
    "s_lshr_b32 s55, s28, 2\n"
    "s_lshl1_add_u32 s55, s31, s55\n"
    "s_lshl_b32 s55, s55, 9\n"
    "s_add_u32 s55, s55, 18104320\n"
    "s_add_u32 s66, s22, s55\n"
    "s_addc_u32 s67, s23, 0\n"
    "s_lshl_b32 s55, s28, 13\n"
    "s_cmp_lt_u32 s28, 5\n"
    "s_cselect_b32 s42, 0, 28672\n"
    "s_add_u32 s42, s42, s55\n"
    "s_add_u32 s42, s42, 57344\n"
    "s_lshl_b32 s55, s28, 12\n"
    "s_add_u32 s56, s55, 98304\n"
    "s_add_u32 s55, s55, 28672\n"
    "s_cmp_lt_u32 s28, 7\n"
    "s_cselect_b32 s43, s55, 151552\n"
    "s_cselect_b32 s44, s56, 155648\n"
    "s_mul_i32 s74, s28, 3584\n"
    "s_mul_i32 s57, s37, 28672\n"
    "s_lshl_b32 s55, s39, 12\n"
    "s_add_u32 s57, s57, s55\n"
    "s_lshl_b32 s55, s91, 10\n"
    "s_add_u32 s57, s57, s55\n"
    "s_add_u32 s57, s57, 3719168\n"
    "s_add_u32 s68, s22, s57\n"
    "s_addc_u32 s69, s23, 0\n"
    "s_lshl1_add_u32 s57, s37, s38\n"
    "s_mul_i32 s55, s57, 112\n"
    "s_mul_i32 s56, s28, 14\n"
    "s_add_u32 s55, s55, s56\n"
    "s_lshl_b32 s55, s55, 9\n"
    "s_add_u32 s55, s55, 5554176\n"
    "s_add_u32 s70, s22, s55\n"
    "s_addc_u32 s71, s23, 0\n"
    "s_mul_i32 s55, s57, 14336\n"
    "s_lshl_b32 s56, s39, 7\n"
    "s_add_u32 s55, s55, s56\n"
    "s_add_u32 s72, s24, s55\n"
    "s_addc_u32 s73, s25, 0\n"
    "s_lshl_b32 s55, s36, 16\n"
    "s_and_b32 s56, s36, 0xffff0000\n"
    "v_bfe_u32 v22, v1, 3, 1\n"
    "v_cmp_eq_u32 vcc, 1, v22\n"
    "v_mov_b32 v19, s55\n"
    "v_mov_b32 v22, s56\n"
    "v_cndmask_b32 v19, v19, v22, vcc\n"
    "s_waitcnt vmcnt(8)\n"
    "v_readfirstlane_b32 s34, v64\n"
    "v_readfirstlane_b32 s35, v65\n"
    "s_add_u32 s34, s34, 0x7F4A7C15\n"
    "s_addc_u32 s35, s35, 0x9E3779B9\n"
    "v_mov_b32 v4, s34\n"
    "v_mov_b32 v5, s35\n"
    "s_cmp_ge_u32 s96, 56\n"
    "s_cselect_b32 s64, 2, 0\n"
    "s_waitcnt vmcnt(0)\n"
    "s_cmp_lg_u32 s64, 0\n"
    "s_cbranch_scc1 .Lk_s66\n"
    "s_waitcnt vmcnt(0)\n"
    "ds_read_b64_tr_b16 v[224:225], v143 offset:0\n"
    "ds_read_b64_tr_b16 v[226:227], v143 offset:512\n"
    "ds_read_b64_tr_b16 v[228:229], v143 offset:32\n"
    "ds_read_b64_tr_b16 v[230:231], v143 offset:544\n"
    "ds_read_b64_tr_b16 v[232:233], v143 offset:64\n"
    "ds_read_b64_tr_b16 v[234:235], v143 offset:576\n"
    "ds_read_b64_tr_b16 v[236:237], v143 offset:96\n"
    "ds_read_b64_tr_b16 v[238:239], v143 offset:608\n"
    "s_mov_b32 s55, s11\n"
    "s_mov_b32 s56, s10\n"
    "s_lshl_b32 s57, s55, 5\n"
    "s_add_u32 s57, s57, s56\n"
    "s_lshl_b32 s57, s57, 4\n"
    "s_add_u32 s57, s57, 18104320\n"
    "s_add_u32 s48, s22, s57\n"
    "s_addc_u32 s49, s23, 0\n"
    "s_mul_i32 s55, s55, 112\n"
    "s_add_u32 s55, s55, s56\n"
    "s_lshl_b32 s55, s55, 10\n"
    "s_add_u32 s55, s55, 13287424\n"
    "s_add_u32 s46, s22, s55\n"
    "s_addc_u32 s47, s23, 0\n"
    "s_waitcnt lgkmcnt(6)\n"
    "global_store_dwordx4 v2, v[224:227], s[46:47] sc1\n"
    "s_add_u32 s46, s46, 28672\n"
    "s_addc_u32 s47, s47, 0\n"
    "s_waitcnt lgkmcnt(4)\n"
    "global_store_dwordx4 v2, v[228:231], s[46:47] sc1\n"
    "s_add_u32 s46, s46, 28672\n"
    "s_addc_u32 s47, s47, 0\n"
    "s_waitcnt lgkmcnt(2)\n"
    "global_store_dwordx4 v2, v[232:235], s[46:47] sc1\n"
    "s_add_u32 s46, s46, 28672\n"
    "s_addc_u32 s47, s47, 0\n"
    "s_waitcnt lgkmcnt(0)\n"
    "global_store_dwordx4 v2, v[236:239], s[46:47] sc1\n"
    "s_mov_b64 s[10:11], s[48:49]\n"
    "s_mov_b32 s64, 1\n"
    ".Lk_s66:\n"
    "v_lshlrev_b32 v66, 16, v32\n"
    "v_and_b32 v67, 0xffff0000, v32\n"
    "v_lshlrev_b32 v68, 16, v33\n"
    "v_and_b32 v69, 0xffff0000, v33\n"
    "v_lshlrev_b32 v70, 16, v34\n"
    "v_and_b32 v71, 0xffff0000, v34\n"
    "v_lshlrev_b32 v72, 16, v35\n"
    "v_and_b32 v73, 0xffff0000, v35\n"
    "v_lshlrev_b32 v74, 16, v36\n"
    "v_and_b32 v75, 0xffff0000, v36\n"
    "v_lshlrev_b32 v76, 16, v37\n"
    "v_and_b32 v77, 0xffff0000, v37\n"
    "v_lshlrev_b32 v78, 16, v38\n"
    "v_and_b32 v79, 0xffff0000, v38\n"
    "v_lshlrev_b32 v80, 16, v39\n"
    "v_and_b32 v81, 0xffff0000, v39\n"
    "v_pk_add_f32 v[84:85], v[66:67], v[68:69]\n"
    "v_pk_add_f32 v[86:87], v[70:71], v[72:73]\n"
    "v_pk_add_f32 v[88:89], v[74:75], v[76:77]\n"
    "v_pk_add_f32 v[90:91], v[78:79], v[80:81]\n"
    "v_pk_add_f32 v[84:85], v[84:85], v[86:87]\n"
    "v_pk_add_f32 v[88:89], v[88:89], v[90:91]\n"
    "v_add_f32 v84, v84, v85\n"
    "v_add_f32 v88, v88, v89\n"
    "v_cndmask_b32 v84, 0, v84, s[62:63]\n"
    "v_cndmask_b32 v88, 0, v88, s[62:63]\n"
    "s_nop 0\n"
    "v_add_f32_dpp v84, v84, v84 quad_perm:[1,0,3,2] row_mask:0xf bank_mask:0xf\n"
    "v_add_f32_dpp v88, v88, v88 quad_perm:[1,0,3,2] row_mask:0xf bank_mask:0xf\n"
    "s_nop 0\n"
    "v_add_f32_dpp v84, v84, v84 quad_perm:[2,3,0,1] row_mask:0xf bank_mask:0xf\n"
    "v_add_f32_dpp v88, v88, v88 quad_perm:[2,3,0,1] row_mask:0xf bank_mask:0xf\n"
    "s_nop 0\n"
    "v_add_f32_dpp v84, v84, v84 row_half_mirror row_mask:0xf bank_mask:0xf\n"
    "v_add_f32_dpp v88, v88, v88 row_half_mirror row_mask:0xf bank_mask:0xf\n"
    "s_nop 0\n"
    "v_add_f32_dpp v84, v84, v84 row_mirror row_mask:0xf bank_mask:0xf\n"
    "v_add_f32_dpp v88, v88, v88 row_mirror row_mask:0xf bank_mask:0xf\n"
    "v_mov_b32 v85, v84\n"
    "v_mov_b32 v89, v88\n"
    "s_nop 0\n"
    "v_permlane16_swap_b32 v84, v85\n"
    "v_permlane16_swap_b32 v88, v89\n"
    "v_add_f32 v84, v84, v85\n"
    "v_add_f32 v88, v88, v89\n"
    "v_mov_b32 v85, v84\n"
    "v_mov_b32 v89, v88\n"
    "s_nop 0\n"
    "v_permlane32_swap_b32 v84, v85\n"
    "v_permlane32_swap_b32 v88, v89\n"
    "v_add_f32 v84, v84, v85\n"
    "v_add_f32 v88, v88, v89\n"
    "v_mul_f32 v84, 0x3b124925, v84\n"
    "v_mul_f32 v88, 0x3b124925, v88\n"
    "v_sub_f32 v66, v66, v84\n"
    "v_sub_f32 v74, v74, v88\n"
    "v_sub_f32 v67, v67, v84\n"
    "v_sub_f32 v75, v75, v88\n"
    "v_sub_f32 v68, v68, v84\n"
    "v_sub_f32 v76, v76, v88\n"
    "v_sub_f32 v69, v69, v84\n"
    "v_sub_f32 v77, v77, v88\n"
    "v_sub_f32 v70, v70, v84\n"
    "v_sub_f32 v78, v78, v88\n"
    "v_sub_f32 v71, v71, v84\n"
    "v_sub_f32 v79, v79, v88\n"
    "v_sub_f32 v72, v72, v84\n"
    "v_sub_f32 v80, v80, v88\n"
    "v_sub_f32 v73, v73, v84\n"
    "v_sub_f32 v81, v81, v88\n"
    "v_mul_f32 v86, v66, v66\n"
    "v_mul_f32 v90, v74, v74\n"
    "v_fmac_f32 v86, v67, v67\n"
    "v_fmac_f32 v90, v75, v75\n"
    "v_fmac_f32 v86, v68, v68\n"
    "v_fmac_f32 v90, v76, v76\n"
    "v_fmac_f32 v86, v69, v69\n"
    "v_fmac_f32 v90, v77, v77\n"
    "v_fmac_f32 v86, v70, v70\n"
    "v_fmac_f32 v90, v78, v78\n"
    "v_fmac_f32 v86, v71, v71\n"
    "v_fmac_f32 v90, v79, v79\n"
    "v_fmac_f32 v86, v72, v72\n"
    "v_fmac_f32 v90, v80, v80\n"
    "v_fmac_f32 v86, v73, v73\n"
    "v_fmac_f32 v90, v81, v81\n"
    "v_cndmask_b32 v86, 0, v86, s[62:63]\n"
    "v_cndmask_b32 v90, 0, v90, s[62:63]\n"
    "s_nop 0\n"
    "v_add_f32_dpp v86, v86, v86 quad_perm:[1,0,3,2] row_mask:0xf bank_mask:0xf\n"
    "v_add_f32_dpp v90, v90, v90 quad_perm:[1,0,3,2] row_mask:0xf bank_mask:0xf\n"
    "s_nop 0\n"
    "v_add_f32_dpp v86, v86, v86 quad_perm:[2,3,0,1] row_mask:0xf bank_mask:0xf\n"
    "v_add_f32_dpp v90, v90, v90 quad_perm:[2,3,0,1] row_mask:0xf bank_mask:0xf\n"
    "s_nop 0\n"
    "v_add_f32_dpp v86, v86, v86 row_half_mirror row_mask:0xf bank_mask:0xf\n"
    "v_add_f32_dpp v90, v90, v90 row_half_mirror row_mask:0xf bank_mask:0xf\n"
    "s_nop 0\n"
    "v_add_f32_dpp v86, v86, v86 row_mirror row_mask:0xf bank_mask:0xf\n"
    "v_add_f32_dpp v90, v90, v90 row_mirror row_mask:0xf bank_mask:0xf\n"
    "v_mov_b32 v87, v86\n"
    "v_mov_b32 v91, v90\n"
    "s_nop 0\n"
    "v_permlane16_swap_b32 v86, v87\n"
    "v_permlane16_swap_b32 v90, v91\n"
    "v_add_f32 v86, v86, v87\n"
    "v_add_f32 v90, v90, v91\n"
    "v_mov_b32 v87, v86\n"
    "v_mov_b32 v91, v90\n"
    "s_nop 0\n"
    "v_permlane32_swap_b32 v86, v87\n"
    "v_permlane32_swap_b32 v90, v91\n"
    "v_add_f32 v86, v86, v87\n"
    "v_add_f32 v90, v90, v91\n"
    "v_mul_f32 v86, 0x3b124925, v86\n"
    "v_mul_f32 v90, 0x3b124925, v90\n"
    "v_cvt_pk_bf16_f32 v86, v86, v86\n"
    "v_and_b32 v86, 0xffff0000, v86\n"
    "v_add_f32 v86, 0x3727c5ac, v86\n"
    "v_cvt_pk_bf16_f32 v86, v86, v86\n"
    "v_and_b32 v86, 0xffff0000, v86\n"
    "v_sqrt_f32 v86, v86\n"
    "s_nop 0\n"
    "v_cvt_pk_bf16_f32 v32, v84, v86\n"
    "v_mov_b32 v33, s34\n"
    "v_cvt_pk_bf16_f32 v90, v90, v90\n"
    "v_and_b32 v90, 0xffff0000, v90\n"
    "v_add_f32 v90, 0x3727c5ac, v90\n"
    "v_cvt_pk_bf16_f32 v90, v90, v90\n"
    "v_and_b32 v90, 0xffff0000, v90\n"
    "v_sqrt_f32 v90, v90\n"
    "s_nop 0\n"
    "v_cvt_pk_bf16_f32 v34, v88, v90\n"
    "v_mov_b32 v35, s34\n"
    "s_lshl_b32 s55, s90, 6\n"
    "s_lshl4_add_u32 s55, s91, s55\n"
    "s_add_u32 s55, s55, 3670016\n"
    "s_add_u32 s46, s22, s55\n"
    "s_addc_u32 s47, s23, 0\n"
    "s_mov_b64 exec, 1\n"
    "global_store_dwordx2 v3, v[32:33], s[46:47] offset:0 sc1\n"
    "global_store_dwordx2 v3, v[34:35], s[46:47] offset:8 sc1\n"
    "s_mov_b64 exec, -1\n"
    "s_mul_i32 s57, s91, 8256\n"
    "s_lshl_b32 s55, s91, 4\n"
    "s_sub_u32 s56, 64, s55\n"
    "v_mov_b32 v40, 0\n"
    "v_mov_b32 v41, 0\n"
    "v_mov_b32 v42, 0\n"
    "v_mov_b32 v43, 0\n"
    "v_mov_b32 v44, 0\n"
    "v_mov_b32 v45, 0\n"
    "s_add_u32 s58, s57, 98560\n"
    "v_mov_b32 v139, s58\n"
    "s_lshl_b64 s[92:93], s[92:93], s56\n"
    "s_cmp_eq_u32 s91, 0\n"
    "s_cselect_b64 s[92:93], 0, s[92:93]\n"
    "v_mov_b32 v46, s92\n"
    "v_mov_b32 v47, s93\n"
    "s_mov_b64 exec, 1\n"
    "ds_write_b128 v139, v[40:43] offset:0\n"
    "ds_write_b128 v139, v[44:47] offset:16\n"
    "s_mov_b64 exec, -1\n"
    "s_lshl_b64 s[94:95], s[94:95], s56\n"
    "s_cmp_eq_u32 s91, 0\n"
    "s_cselect_b64 s[94:95], 0, s[94:95]\n"
    "v_mov_b32 v46, s94\n"
    "v_mov_b32 v47, s95\n"
    "s_mov_b64 exec, 1\n"
    "ds_write_b128 v139, v[40:43] offset:4128\n"
    "ds_write_b128 v139, v[44:47] offset:4144\n"
    "s_mov_b64 exec, -1\n"
    "ds_write_b128 v138, v[96:99] offset:0\n"
    "v_alignbit_b32 v112, v97, v96, 16\n"
    "v_alignbit_b32 v113, v98, v97, 16\n"
    "v_alignbit_b32 v114, v99, v98, 16\n"
    "v_alignbit_b32 v115, v100, v99, 16\n"
    "ds_write_b128 v138, v[112:115] offset:8256\n"
    "v_mov_b32 v116, v97\n"
    "v_mov_b32 v117, v98\n"
    "v_mov_b32 v118, v99\n"
    "v_mov_b32 v119, v100\n"
    "ds_write_b128 v138, v[116:119] offset:16512\n"
    "v_alignbit_b32 v120, v98, v97, 16\n"
    "v_alignbit_b32 v121, v99, v98, 16\n"
    "v_alignbit_b32 v122, v100, v99, 16\n"
    "v_alignbit_b32 v123, v101, v100, 16\n"
    "ds_write_b128 v138, v[120:123] offset:24768\n"
    "ds_write_b128 v138, v[104:107] offset:1024\n"
    "v_alignbit_b32 v124, v105, v104, 16\n"
    "v_alignbit_b32 v125, v106, v105, 16\n"
    "v_alignbit_b32 v126, v107, v106, 16\n"
    "v_alignbit_b32 v127, v108, v107, 16\n"
    "ds_write_b128 v138, v[124:127] offset:9280\n"
    "v_mov_b32 v128, v105\n"
    "v_mov_b32 v129, v106\n"
    "v_mov_b32 v130, v107\n"
    "v_mov_b32 v131, v108\n"
    "ds_write_b128 v138, v[128:131] offset:17536\n"
    "v_alignbit_b32 v132, v106, v105, 16\n"
    "v_alignbit_b32 v133, v107, v106, 16\n"
    "v_alignbit_b32 v134, v108, v107, 16\n"
    "v_alignbit_b32 v135, v109, v108, 16\n"
    "ds_write_b128 v138, v[132:135] offset:25792\n"
    "s_and_b32 s55, s91, 1\n"
    "s_lshl_b32 s55, s55, 10\n"
    "s_add_u32 s55, s55, 147968\n"
    "v_add_u32 v139, s55, v2\n"
    "ds_write_b128 v139, v[40:43]\n"
    "s_cmp_lg_u32 s64, 1\n"
    "s_cbranch_scc1 .Lk_s67\n"
    "s_waitcnt vmcnt(2)\n"
    "s_mov_b64 exec, 1\n"
    "global_store_dwordx2 v3, v[4:5], s[10:11] sc1\n"
    "s_mov_b64 exec, -1\n"
    "s_mov_b32 s64, 2\n"
    ".Lk_s67:\n"
    "s_mov_b32 s96, 0\n"
    "s_waitcnt lgkmcnt(0)\n"
    "s_barrier\n"
    "ds_read_b128 v[36:39], v11 offset:14336\n"
    "ds_read_b64 v[32:33], v10 offset:0\n"
    "ds_read_b64 v[34:35], v10 offset:8\n"
    "ds_read_b128 v[44:47], v11 offset:12288\n"
    "ds_read_b64 v[40:41], v10 offset:512\n"
    "ds_read_b64 v[42:43], v10 offset:520\n"
    "ds_read_b128 v[84:87], v12 offset:14336\n"
    "ds_read_b64 v[80:81], v10 offset:64\n"
    "ds_read_b64 v[82:83], v10 offset:72\n"
    "ds_read_b128 v[92:95], v12 offset:12288\n"
    "ds_read_b64 v[88:89], v10 offset:576\n"
    "ds_read_b64 v[90:91], v10 offset:584\n"
    "global_load_dwordx2 v[20:21], v8, s[66:67] sc1\n"
    "s_waitcnt lgkmcnt(8)\n"
    "v_mfma_f32_16x16x32_bf16 v[228:231], v[32:35], v[44:47], 0\n"
    "v_mfma_f32_16x16x32_bf16 v[224:227], v[32:35], v[36:39], 0\n"
    "s_waitcnt lgkmcnt(6)\n"
    "v_mfma_f32_16x16x32_bf16 v[228:231], v[40:43], v[36:39], v[228:231]\n"
    "s_waitcnt lgkmcnt(2)\n"
    "v_mfma_f32_16x16x32_bf16 v[228:231], v[80:83], v[92:95], v[228:231]\n"
    "v_mfma_f32_16x16x32_bf16 v[224:227], v[80:83], v[84:87], v[224:227]\n"
    "s_waitcnt lgkmcnt(0)\n"
    "v_mfma_f32_16x16x32_bf16 v[228:231], v[88:91], v[84:87], v[228:231]\n"
    "s_nop 5\n"
    "ds_write_b128 v13, v[224:227] offset:0\n"
    "s_nop 0\n"
    "ds_write_b128 v13, v[228:231] offset:1024\n"
    "ds_read_b128 v[52:55], v11 offset:10240\n"
    "ds_read_b64 v[48:49], v10 offset:1024\n"
    "ds_read_b64 v[50:51], v10 offset:1032\n"
    "ds_read_b128 v[60:63], v11 offset:8192\n"
    "ds_read_b64 v[56:57], v10 offset:1536\n"
    "ds_read_b64 v[58:59], v10 offset:1544\n"
    "ds_read_b128 v[100:103], v12 offset:10240\n"
    "ds_read_b64 v[96:97], v10 offset:1088\n"
    "ds_read_b64 v[98:99], v10 offset:1096\n"
    "ds_read_b128 v[108:111], v12 offset:8192\n"
    "ds_read_b64 v[104:105], v10 offset:1600\n"
    "ds_read_b64 v[106:107], v10 offset:1608\n"
    "ds_read_b64 v[252:253], v15 offset:8192\n"
    "ds_read_b64 v[254:255], v16 offset:8192\n"
    "s_waitcnt lgkmcnt(14)\n"
    "s_barrier\n"
    "s_add_u32 s50, s22, 3674112\n"
    "s_addc_u32 s51, s23, 0\n"
    "global_load_dwordx4 v[240:243], v18, s[50:51] offset:0 sc1\n"
    "global_load_dwordx4 v[244:247], v18, s[50:51] offset:16 sc1\n"
    "v_mfma_f32_16x16x32_bf16 v[232:235], v[40:43], v[44:47], 0\n"
    "s_waitcnt vmcnt(2)\n"
    "v_cmp_ne_u64 vcc, s[34:35], v[20:21]\n"
    "s_cbranch_vccnz .Lk_r68\n"
    ".Lk_c69:\n"
    "v_mfma_f32_16x16x32_bf16 v[232:235], v[88:91], v[92:95], v[232:235]\n"
    "ds_read_b128 v[68:71], v11 offset:6144\n"
    "ds_read_b64 v[64:65], v10 offset:2048\n"
    "ds_read_b64 v[66:67], v10 offset:2056\n"
    "ds_read_b128 v[76:79], v11 offset:4096\n"
    "ds_read_b64 v[72:73], v10 offset:2560\n"
    "ds_read_b64 v[74:75], v10 offset:2568\n"
    "ds_read_b128 v[116:119], v12 offset:6144\n"
    "ds_read_b64 v[112:113], v10 offset:2112\n"
    "ds_read_b64 v[114:115], v10 offset:2120\n"
    "ds_read_b128 v[124:127], v12 offset:4096\n"
    "ds_read_b64 v[120:121], v10 offset:2624\n"
    "ds_read_b64 v[122:123], v10 offset:2632\n"
    "s_waitcnt lgkmcnt(15)\n"
    "v_mfma_f32_16x16x32_bf16 v[236:239], v[32:35], v[60:63], 0\n"
    "v_mfma_f32_16x16x32_bf16 v[232:235], v[32:35], v[52:55], v[232:235]\n"
    "v_mfma_f32_16x16x32_bf16 v[236:239], v[40:43], v[52:55], v[236:239]\n"
    "v_mfma_f32_16x16x32_bf16 v[236:239], v[48:51], v[44:47], v[236:239]\n"
    "v_mfma_f32_16x16x32_bf16 v[232:235], v[48:51], v[36:39], v[232:235]\n"
    "v_mfma_f32_16x16x32_bf16 v[236:239], v[56:59], v[36:39], v[236:239]\n"
    "v_mfma_f32_16x16x32_bf16 v[236:239], v[80:83], v[108:111], v[236:239]\n"
    "v_mfma_f32_16x16x32_bf16 v[232:235], v[80:83], v[100:103], v[232:235]\n"
    "v_mfma_f32_16x16x32_bf16 v[236:239], v[88:91], v[100:103], v[236:239]\n"
    "v_mfma_f32_16x16x32_bf16 v[236:239], v[96:99], v[92:95], v[236:239]\n"
    "v_mfma_f32_16x16x32_bf16 v[232:235], v[96:99], v[84:87], v[232:235]\n"
    "s_waitcnt lgkmcnt(14)\n"
    "v_mfma_f32_16x16x32_bf16 v[236:239], v[104:107], v[84:87], v[236:239]\n"
    "s_nop 5\n"
    "ds_write_b128 v13, v[232:235] offset:16384\n"
    "s_nop 0\n"
    "ds_write_b128 v13, v[236:239] offset:17408\n"
    "v_mfma_f32_16x16x32_bf16 v[224:227], v[40:43], v[60:63], 0\n"
    "v_mfma_f32_16x16x32_bf16 v[228:231], v[48:51], v[60:63], 0\n"
    "s_waitcnt vmcnt(1)\n"
    "v_bitop3_b32 v241, v241, v243, s34 bitop3:0x7e\n"
    "s_waitcnt vmcnt(0)\n"
    "v_bitop3_b32 v245, v245, v247, s34 bitop3:0x7e\n"
    "v_or_b32 v241, v241, v245\n"
    "v_cmp_ne_u32 vcc, 0, v241\n"
    "s_cbranch_vccnz .Lk_r71\n"
    ".Lk_c72:\n"
    "v_and_b32 v241, 0xffff0000, v240\n"
    "v_and_b32 v243, 0xffff0000, v242\n"
    "v_and_b32 v245, 0xffff0000, v244\n"
    "v_and_b32 v247, 0xffff0000, v246\n"
    "v_rcp_f32 v241, v241\n"
    "v_lshlrev_b32 v240, 16, v240\n"
    "v_rcp_f32 v243, v243\n"
    "v_lshlrev_b32 v242, 16, v242\n"
    "v_rcp_f32 v245, v245\n"
    "v_lshlrev_b32 v244, 16, v244\n"
    "v_rcp_f32 v247, v247\n"
    "v_lshlrev_b32 v246, 16, v246\n"
    "v_mfma_f32_16x16x32_bf16 v[224:227], v[48:51], v[52:55], v[224:227]\n"
    "v_mfma_f32_16x16x32_bf16 v[228:231], v[56:59], v[52:55], v[228:231]\n"
    "s_waitcnt lgkmcnt(14)\n"
    "v_lshlrev_b32 v248, 16, v254\n"
    "v_and_b32 v249, 0xffff0000, v254\n"
    "v_lshlrev_b32 v250, 16, v255\n"
    "v_and_b32 v251, 0xffff0000, v255\n"
    "v_sub_f32 v240, v248, v240\n"
    "v_sub_f32 v242, v249, v242\n"
    "v_sub_f32 v244, v250, v244\n"
    "v_sub_f32 v246, v251, v246\n"
    "v_mul_f32 v240, v240, v241\n"
    "v_mul_f32 v241, v242, v243\n"
    "v_mul_f32 v242, v244, v245\n"
    "v_mul_f32 v243, v246, v247\n"
    "v_and_b32 v244, 0xffff0000, v253\n"
    "v_lshlrev_b32 v245, 16, v253\n"
    "v_and_b32 v246, 0xffff0000, v252\n"
    "v_lshlrev_b32 v247, 16, v252\n"
    "v_mul_f32 v244, v244, v19\n"
    "v_mul_f32 v245, v245, v19\n"
    "v_mul_f32 v246, v246, v19\n"
    "v_mul_f32 v247, v247, v19\n"
    "s_lshl_b32 s55, s29, 7\n"
    "s_add_u32 s55, s55, 4177920\n"
    "s_add_u32 s46, s22, s55\n"
    "s_addc_u32 s47, s23, 0\n"
    "v_mfma_f32_16x16x32_bf16 v[224:227], v[56:59], v[44:47], v[224:227]\n"
    "v_mfma_f32_16x16x32_bf16 v[224:227], v[88:91], v[108:111], v[224:227]\n"
    "s_waitcnt lgkmcnt(0)\n"
    "s_barrier\n"
    "ds_read_b128 v[252:255], v14 offset:16384\n"
    "ds_read_b128 v[232:235], v14 offset:18432\n"
    "ds_read_b128 v[236:239], v14 offset:20480\n"
    "ds_read_b128 v[28:31], v14 offset:22528\n"
    "ds_read_b128 v[132:135], v11 offset:2048\n"
    "ds_read_b64 v[128:129], v10 offset:3072\n"
    "ds_read_b64 v[130:131], v10 offset:3080\n"
    "ds_read_b128 v[140:143], v11 offset:0\n"
    "ds_read_b64 v[136:137], v10 offset:3584\n"
    "ds_read_b64 v[138:139], v10 offset:3592\n"
    "v_mfma_f32_16x16x32_bf16 v[228:231], v[96:99], v[108:111], v[228:231]\n"
    "v_mfma_f32_16x16x32_bf16 v[224:227], v[96:99], v[100:103], v[224:227]\n"
    "v_mfma_f32_16x16x32_bf16 v[228:231], v[104:107], v[100:103], v[228:231]\n"
    "v_mfma_f32_16x16x32_bf16 v[224:227], v[104:107], v[92:95], v[224:227]\n"
    "s_waitcnt lgkmcnt(9)\n"
    "v_pk_add_f32 v[252:253], v[252:253], v[244:245]\n"
    "v_pk_add_f32 v[254:255], v[254:255], v[246:247]\n"
    "s_waitcnt lgkmcnt(8)\n"
    "v_pk_add_f32 v[252:253], v[252:253], v[232:233]\n"
    "v_pk_add_f32 v[254:255], v[254:255], v[234:235]\n"
    "s_waitcnt lgkmcnt(7)\n"
    "v_pk_add_f32 v[252:253], v[252:253], v[236:237]\n"
    "v_pk_add_f32 v[254:255], v[254:255], v[238:239]\n"
    "s_waitcnt lgkmcnt(6)\n"
    "v_pk_add_f32 v[252:253], v[252:253], v[28:29]\n"
    "v_pk_add_f32 v[254:255], v[254:255], v[30:31]\n"
    "v_pk_fma_f32 v[248:249], v[240:241], v[252:253], v[248:249]\n"
    "v_pk_fma_f32 v[250:251], v[242:243], v[254:255], v[250:251]\n"
    "v_cvt_pk_bf16_f32 v24, v248, v249\n"
    "v_cvt_pk_bf16_f32 v25, v250, v251\n"
    "global_store_dwordx2 v17, v[24:25], s[46:47] sc1\n"
    "s_mov_b32 s75, 0\n"
    "s_add_u32 s50, s22, 3682304\n"
    "s_addc_u32 s51, s23, 0\n"
    "global_load_dwordx4 v[240:243], v18, s[50:51] offset:0 sc1\n"
    "global_load_dwordx4 v[244:247], v18, s[50:51] offset:16 sc1\n"
    "ds_read_b64 v[252:253], v15 offset:0\n"
    "ds_read_b64 v[254:255], v16 offset:0\n"
    "v_min_u32 v22, 27, v1\n"
    "s_mul_i32 s55, s28, 28\n"
    "v_add_lshl_u32 v7, v22, s55, 4\n"
    "s_add_u32 s94, s8, 0\n"
    "s_addc_u32 s95, s9, 0\n"
    "global_load_dwordx4 v[144:147], v2, s[94:95] offset:0 sc1\n"
    "v_mfma_f32_16x16x32_bf16 v[228:231], v[32:35], v[76:79], v[228:231]\n"
    "v_mfma_f32_16x16x32_bf16 v[224:227], v[32:35], v[68:71], v[224:227]\n"
    "v_mfma_f32_16x16x32_bf16 v[228:231], v[40:43], v[68:71], v[228:231]\n"
    "global_load_dwordx4 v[148:151], v2, s[94:95] offset:1024 sc1\n"
    "v_mfma_f32_16x16x32_bf16 v[228:231], v[64:67], v[44:47], v[228:231]\n"
    "v_mfma_f32_16x16x32_bf16 v[224:227], v[64:67], v[36:39], v[224:227]\n"
    "v_mfma_f32_16x16x32_bf16 v[228:231], v[72:75], v[36:39], v[228:231]\n"
    "global_load_dwordx4 v[152:155], v2, s[94:95] offset:2048 sc1\n"
    "v_mfma_f32_16x16x32_bf16 v[228:231], v[80:83], v[124:127], v[228:231]\n"
    "v_mfma_f32_16x16x32_bf16 v[224:227], v[80:83], v[116:119], v[224:227]\n"
    "v_mfma_f32_16x16x32_bf16 v[228:231], v[88:91], v[116:119], v[228:231]\n"
    "global_load_dwordx4 v[156:159], v2, s[94:95] offset:3072 sc1\n"
    "v_mfma_f32_16x16x32_bf16 v[228:231], v[112:115], v[92:95], v[228:231]\n"
    "v_mfma_f32_16x16x32_bf16 v[224:227], v[112:115], v[84:87], v[224:227]\n"
    "v_mfma_f32_16x16x32_bf16 v[228:231], v[120:123], v[84:87], v[228:231]\n"
    "s_nop 6\n"
    "ds_write_b128 v13, v[224:227] offset:0\n"
    "ds_write_b128 v13, v[228:231] offset:1024\n"
    "s_add_u32 s94, s8, 4096\n"
    "s_addc_u32 s95, s9, 0\n"
    "global_load_dwordx4 v[160:163], v2, s[94:95] offset:0 sc1\n"
    "s_waitcnt lgkmcnt(6)\n"
    "v_mfma_f32_16x16x32_bf16 v[236:239], v[32:35], v[140:143], 0\n"
    "v_mfma_f32_16x16x32_bf16 v[232:235], v[32:35], v[132:135], 0\n"
    "s_getreg_b32 s55, hwreg(HW_REG_IB_STS, 0, 4)\n"
    "s_or_b32 s55, s55, s75\n"
    "s_cmp_gt_u32 s55, 7\n"
    "s_cbranch_scc1 .Lk_s74\n"
    "v_lshlrev_b32 v22, 12, v1\n"
    "s_add_u32 s48, s98, 32768\n"
    "s_addc_u32 s49, s99, 0\n"
    "s_waitcnt vmcnt(7)\n"
    "s_mov_b64 exec, 3\n"
    "global_store_dwordx2 v22, v[4:5], s[48:49] sc1\n"
    "s_mov_b64 exec, -1\n"
    "s_mov_b32 s75, 64\n"
    ".Lk_s74:\n"
    "v_mfma_f32_16x16x32_bf16 v[236:239], v[40:43], v[132:135], v[236:239]\n"
    "global_load_dwordx4 v[164:167], v2, s[94:95] offset:1024 sc1\n"
    "v_mfma_f32_16x16x32_bf16 v[236:239], v[128:131], v[44:47], v[236:239]\n"
    "v_mfma_f32_16x16x32_bf16 v[232:235], v[128:131], v[36:39], v[232:235]\n"
    "s_waitcnt lgkmcnt(4)\n"
    "v_mfma_f32_16x16x32_bf16 v[236:239], v[136:139], v[36:39], v[236:239]\n"
    "ds_read_b128 v[132:135], v12 offset:2048\n"
    "ds_read_b64 v[128:129], v10 offset:3136\n"
    "ds_read_b64 v[130:131], v10 offset:3144\n"
    "ds_read_b128 v[140:143], v12 offset:0\n"
    "ds_read_b64 v[136:137], v10 offset:3648\n"
    "ds_read_b64 v[138:139], v10 offset:3656\n"
    "s_waitcnt lgkmcnt(6)\n"
    "s_barrier\n"
    "s_getreg_b32 s55, hwreg(HW_REG_IB_STS, 0, 4)\n"
    "s_or_b32 s55, s55, s75\n"
    "s_cmp_gt_u32 s55, 8\n"
    "s_cbranch_scc1 .Lk_s75\n"
    "v_lshlrev_b32 v22, 12, v1\n"
    "s_add_u32 s48, s98, 32768\n"
    "s_addc_u32 s49, s99, 0\n"
    "s_waitcnt vmcnt(8)\n"
    "s_mov_b64 exec, 3\n"
    "global_store_dwordx2 v22, v[4:5], s[48:49] sc1\n"
    "s_mov_b64 exec, -1\n"
    "s_mov_b32 s75, 64\n"
    ".Lk_s75:\n"
    "v_mfma_f32_16x16x32_bf16 v[232:235], v[40:43], v[76:79], v[232:235]\n"
    "global_load_dwordx4 v[168:171], v2, s[94:95] offset:2048 sc1\n"
    "v_mfma_f32_16x16x32_bf16 v[236:239], v[48:51], v[76:79], v[236:239]\n"
    "v_mfma_f32_16x16x32_bf16 v[232:235], v[48:51], v[68:71], v[232:235]\n"
    "v_mfma_f32_16x16x32_bf16 v[236:239], v[56:59], v[68:71], v[236:239]\n"
    "s_getreg_b32 s55, hwreg(HW_REG_IB_STS, 0, 4)\n"
    "s_or_b32 s55, s55, s75\n"
    "s_cmp_gt_u32 s55, 9\n"
    "s_cbranch_scc1 .Lk_s76\n"
    "v_lshlrev_b32 v22, 12, v1\n"
    "s_add_u32 s48, s98, 32768\n"
    "s_addc_u32 s49, s99, 0\n"
    "s_waitcnt vmcnt(9)\n"
    "s_mov_b64 exec, 3\n"
    "global_store_dwordx2 v22, v[4:5], s[48:49] sc1\n"
    "s_mov_b64 exec, -1\n"
    "s_mov_b32 s75, 64\n"
    ".Lk_s76:\n"
    "v_mfma_f32_16x16x32_bf16 v[232:235], v[56:59], v[60:63], v[232:235]\n"
    "global_load_dwordx4 v[172:175], v2, s[94:95] offset:3072 sc1\n"
    "v_mfma_f32_16x16x32_bf16 v[236:239], v[64:67], v[60:63], v[236:239]\n"
    "v_mfma_f32_16x16x32_bf16 v[232:235], v[64:67], v[52:55], v[232:235]\n"
    "v_mfma_f32_16x16x32_bf16 v[236:239], v[72:75], v[52:55], v[236:239]\n"
    "s_getreg_b32 s55, hwreg(HW_REG_IB_STS, 0, 4)\n"
    "s_or_b32 s55, s55, s75\n"
    "s_cmp_gt_u32 s55, 10\n"
    "s_cbranch_scc1 .Lk_s77\n"
    "v_lshlrev_b32 v22, 12, v1\n"
    "s_add_u32 s48, s98, 32768\n"
    "s_addc_u32 s49, s99, 0\n"
    "s_waitcnt vmcnt(10)\n"
    "s_mov_b64 exec, 3\n"
    "global_store_dwordx2 v22, v[4:5], s[48:49] sc1\n"
    "s_mov_b64 exec, -1\n"
    "s_mov_b32 s75, 64\n"
    ".Lk_s77:\n"
    "v_mfma_f32_16x16x32_bf16 v[232:235], v[72:75], v[44:47], v[232:235]\n"
    "s_add_u32 s94, s8, 8192\n"
    "s_addc_u32 s95, s9, 0\n"
    "global_load_dwordx4 v[176:179], v2, s[94:95] offset:0 sc1\n"
    "v_mfma_f32_16x16x32_bf16 v[232:235], v[88:91], v[124:127], v[232:235]\n"
    "v_mfma_f32_16x16x32_bf16 v[236:239], v[96:99], v[124:127], v[236:239]\n"
    "v_mfma_f32_16x16x32_bf16 v[232:235], v[96:99], v[116:119], v[232:235]\n"
    "s_getreg_b32 s55, hwreg(HW_REG_IB_STS, 0, 4)\n"
    "s_or_b32 s55, s55, s75\n"
    "s_cmp_gt_u32 s55, 11\n"
    "s_cbranch_scc1 .Lk_s78\n"
    "v_lshlrev_b32 v22, 12, v1\n"
    "s_add_u32 s48, s98, 32768\n"
    "s_addc_u32 s49, s99, 0\n"
    "s_waitcnt vmcnt(11)\n"
    "s_mov_b64 exec, 3\n"
    "global_store_dwordx2 v22, v[4:5], s[48:49] sc1\n"
    "s_mov_b64 exec, -1\n"
    "s_mov_b32 s75, 64\n"
    ".Lk_s78:\n"
    "v_mfma_f32_16x16x32_bf16 v[236:239], v[104:107], v[116:119], v[236:239]\n"
    "global_load_dwordx4 v[180:183], v2, s[94:95] offset:1024 sc1\n"
    "v_mfma_f32_16x16x32_bf16 v[232:235], v[104:107], v[108:111], v[232:235]\n"
    "v_mfma_f32_16x16x32_bf16 v[236:239], v[112:115], v[108:111], v[236:239]\n"
    "v_mfma_f32_16x16x32_bf16 v[232:235], v[112:115], v[100:103], v[232:235]\n"
    "s_getreg_b32 s55, hwreg(HW_REG_IB_STS, 0, 4)\n"
    "s_or_b32 s55, s55, s75\n"
    "s_cmp_gt_u32 s55, 12\n"
    "s_cbranch_scc1 .Lk_s79\n"
    "v_lshlrev_b32 v22, 12, v1\n"
    "s_add_u32 s48, s98, 32768\n"
    "s_addc_u32 s49, s99, 0\n"
    "s_waitcnt vmcnt(12)\n"
    "s_mov_b64 exec, 3\n"
    "global_store_dwordx2 v22, v[4:5], s[48:49] sc1\n"
    "s_mov_b64 exec, -1\n"
    "s_mov_b32 s75, 64\n"
    ".Lk_s79:\n"
    "v_mfma_f32_16x16x32_bf16 v[236:239], v[120:123], v[100:103], v[236:239]\n"
    "global_load_dwordx4 v[184:187], v2, s[94:95] offset:2048 sc1\n"
    "v_mfma_f32_16x16x32_bf16 v[232:235], v[120:123], v[92:95], v[232:235]\n"
    "s_cmp_lg_u32 s75, 0\n"
    "s_cbranch_scc1 .Lk_s80\n"
    "v_lshlrev_b32 v22, 12, v1\n"
    "s_add_u32 s48, s98, 32768\n"
    "s_addc_u32 s49, s99, 0\n"
    "s_waitcnt vmcnt(13)\n"
    "s_mov_b64 exec, 3\n"
    "global_store_dwordx2 v22, v[4:5], s[48:49] sc1\n"
    "s_mov_b64 exec, -1\n"
    "s_mov_b32 s75, 64\n"
    ".Lk_s80:\n"
    "s_waitcnt vmcnt(12)\n"
    "v_bitop3_b32 v241, v241, v243, s34 bitop3:0x7e\n"
    "s_waitcnt vmcnt(11)\n"
    "v_bitop3_b32 v245, v245, v247, s34 bitop3:0x7e\n"
    "v_or_b32 v241, v241, v245\n"
    "v_cmp_ne_u32 vcc, 0, v241\n"
    "s_cbranch_vccnz .Lk_r81\n"
    ".Lk_c82:\n"
    "v_and_b32 v241, 0xffff0000, v240\n"
    "v_and_b32 v243, 0xffff0000, v242\n"
    "v_and_b32 v245, 0xffff0000, v244\n"
    "v_and_b32 v247, 0xffff0000, v246\n"
    "v_rcp_f32 v241, v241\n"
    "v_lshlrev_b32 v240, 16, v240\n"
    "v_rcp_f32 v243, v243\n"
    "v_lshlrev_b32 v242, 16, v242\n"
    "v_rcp_f32 v245, v245\n"
    "v_lshlrev_b32 v244, 16, v244\n"
    "global_load_dwordx4 v[188:191], v2, s[94:95] offset:3072 sc1\n"
    "v_rcp_f32 v247, v247\n"
    "v_lshlrev_b32 v246, 16, v246\n"
    "v_lshlrev_b32 v248, 16, v254\n"
    "v_and_b32 v249, 0xffff0000, v254\n"
    "v_lshlrev_b32 v250, 16, v255\n"
    "v_and_b32 v251, 0xffff0000, v255\n"
    "v_sub_f32 v240, v248, v240\n"
    "v_sub_f32 v242, v249, v242\n"
    "v_sub_f32 v244, v250, v244\n"
    "v_sub_f32 v246, v251, v246\n"
    "v_mul_f32 v240, v240, v241\n"
    "v_mul_f32 v241, v242, v243\n"
    "v_mul_f32 v242, v244, v245\n"
    "v_mul_f32 v243, v246, v247\n"
    "v_and_b32 v244, 0xffff0000, v253\n"
    "v_lshlrev_b32 v245, 16, v253\n"
    "v_and_b32 v246, 0xffff0000, v252\n"
    "v_lshlrev_b32 v247, 16, v252\n"
    "s_add_u32 s94, s8, 12288\n"
    "s_addc_u32 s95, s9, 0\n"
    "global_load_dwordx4 v[192:195], v2, s[94:95] offset:0 sc1\n"
    "v_mul_f32 v244, v244, v19\n"
    "v_mul_f32 v245, v245, v19\n"
    "v_mul_f32 v246, v246, v19\n"
    "v_mul_f32 v247, v247, v19\n"
    "s_lshl_b32 s55, s29, 7\n"
    "s_add_u32 s55, s55, 5095424\n"
    "s_add_u32 s46, s22, s55\n"
    "s_addc_u32 s47, s23, 0\n"
    "s_waitcnt lgkmcnt(2)\n"
    "v_mfma_f32_16x16x32_bf16 v[236:239], v[80:83], v[140:143], v[236:239]\n"
    "global_load_dwordx4 v[196:199], v2, s[94:95] offset:1024 sc1\n"
    "v_mfma_f32_16x16x32_bf16 v[232:235], v[80:83], v[132:135], v[232:235]\n"
    "v_mfma_f32_16x16x32_bf16 v[236:239], v[88:91], v[132:135], v[236:239]\n"
    "v_mfma_f32_16x16x32_bf16 v[236:239], v[128:131], v[92:95], v[236:239]\n"
    "global_load_dwordx4 v[200:203], v2, s[94:95] offset:2048 sc1\n"
    "v_mfma_f32_16x16x32_bf16 v[232:235], v[128:131], v[84:87], v[232:235]\n"
    "s_waitcnt lgkmcnt(0)\n"
    "v_mfma_f32_16x16x32_bf16 v[236:239], v[136:139], v[84:87], v[236:239]\n"
    "s_nop 5\n"
    "ds_write_b128 v13, v[232:235] offset:16384\n"
    "s_nop 0\n"
    "ds_write_b128 v13, v[236:239] offset:17408\n"
    "global_load_dwordx4 v[204:207], v2, s[94:95] offset:3072 sc1\n"
    "s_add_u32 s94, s8, 16384\n"
    "s_addc_u32 s95, s9, 0\n"
    "global_load_dwordx4 v[208:211], v2, s[94:95] offset:0 sc1\n"
    "global_load_dwordx4 v[212:215], v2, s[94:95] offset:1024 sc1\n"
    "global_load_dwordx4 v[216:219], v2, s[94:95] offset:2048 sc1\n"
    "global_load_dwordx4 v[220:223], v2, s[94:95] offset:3072 sc1\n"
    "s_add_u32 s60, s78, 32768\n"
    "s_addc_u32 s61, s79, 0\n"
    "global_load_dwordx2 v[26:27], v7, s[60:61] sc1\n"
    "v_min_u32 v6, 6, v1\n"
    "v_lshlrev_b32 v9, 4, v6\n"
    "s_add_u32 s60, s22, s97\n"
    "s_addc_u32 s61, s23, 0\n"
    "global_load_dwordx2 v[20:21], v9, s[60:61] sc1\n"
    "s_waitcnt lgkmcnt(0)\n"
    "s_barrier\n"
    "ds_read_b128 v[252:255], v14 offset:16384\n"
    "ds_read_b128 v[232:235], v14 offset:18432\n"
    "ds_read_b128 v[236:239], v14 offset:20480\n"
    "ds_read_b128 v[28:31], v14 offset:22528\n"
    "s_waitcnt lgkmcnt(3)\n"
    "v_pk_add_f32 v[252:253], v[252:253], v[244:245]\n"
    "v_pk_add_f32 v[254:255], v[254:255], v[246:247]\n"
    "s_waitcnt lgkmcnt(2)\n"
    "v_pk_add_f32 v[252:253], v[252:253], v[232:233]\n"
    "v_pk_add_f32 v[254:255], v[254:255], v[234:235]\n"
    "s_waitcnt lgkmcnt(1)\n"
    "v_pk_add_f32 v[252:253], v[252:253], v[236:237]\n"
    "v_pk_add_f32 v[254:255], v[254:255], v[238:239]\n"
    "s_waitcnt lgkmcnt(0)\n"
    "v_pk_add_f32 v[252:253], v[252:253], v[28:29]\n"
    "v_pk_add_f32 v[254:255], v[254:255], v[30:31]\n"
    "v_pk_fma_f32 v[248:249], v[240:241], v[252:253], v[248:249]\n"
    "v_pk_fma_f32 v[250:251], v[242:243], v[254:255], v[250:251]\n"
    "v_cvt_pk_bf16_f32 v24, v248, v249\n"
    "v_cvt_pk_bf16_f32 v25, v250, v251\n"
    "global_store_dwordx2 v17, v[24:25], s[46:47] sc1\n"
    "s_mov_b32 s75, 0\n"
    "v_bfe_u32 v10, v1, 2, 2\n"
    "v_lshrrev_b32 v11, 5, v1\n"
    "v_lshl_add_u32 v10, v11, 3, v10\n"
    "v_lshlrev_b32 v10, 6, v10\n"
    "v_bfe_u32 v12, v1, 4, 1\n"
    "v_lshl_or_b32 v10, v12, 5, v10\n"
    "v_and_b32 v13, 3, v1\n"
    "v_lshl_or_b32 v6, v13, 3, v10\n"
    "v_add_u32 v19, 98304, v6\n"
    "s_add_u32 s80, s80, s74\n"
    "s_addc_u32 s81, s81, 0\n"
    "s_sleep 2\n"
    "s_add_u32 s94, s8, 20480\n"
    "s_addc_u32 s95, s9, 0\n"
    "global_load_dwordx4 v[224:227], v2, s[94:95] offset:0 sc1\n"
    "global_load_dwordx4 v[228:231], v2, s[94:95] offset:1024 sc1\n"
    "global_load_dwordx4 v[232:235], v2, s[94:95] offset:2048 sc1\n"
    "global_load_dwordx4 v[236:239], v2, s[94:95] offset:3072 sc1\n"
    "s_waitcnt vmcnt(5)\n"
    "s_add_u32 s60, s22, s97\n"
    "s_addc_u32 s61, s23, 0\n"
    "v_cmp_ne_u64 vcc, s[34:35], v[20:21]\n"
    "s_cbranch_vccnz .Lk_r84\n"
    ".Lk_c85:\n"
    "s_add_u32 s60, s78, 32768\n"
    "s_addc_u32 s61, s79, 0\n"
    "v_cmp_ne_u64 vcc, s[34:35], v[26:27]\n"
    "s_cbranch_vccnz .Lk_r87\n"
    ".Lk_c88:\n"
    ".Lk_G1:\n"
    "s_barrier\n"
    "ds_read_b64_tr_b16 v[96:97], v6 offset:0\n"
    "ds_read_b64_tr_b16 v[98:99], v6 offset:256\n"
    "ds_read_b64_tr_b16 v[100:101], v6 offset:1024\n"
    "ds_read_b64_tr_b16 v[102:103], v6 offset:1280\n"
    "ds_read_b64_tr_b16 v[104:105], v6 offset:2048\n"
    "ds_read_b64_tr_b16 v[106:107], v6 offset:2304\n"
    "ds_read_b64_tr_b16 v[108:109], v6 offset:3072\n"
    "ds_read_b64_tr_b16 v[110:111], v6 offset:3328\n"
    "s_waitcnt lgkmcnt(6)\n"
    "v_mfma_f32_32x32x16_bf16 v[112:127], v[144:147], v[96:99], 0\n"
    "s_add_u32 m0, s74, 28672\n"
    "s_add_u32 s92, s80, 458752\n"
    "s_addc_u32 s93, s81, 0\n"
    "global_load_lds_dwordx4 v2, s[92:93] offset:0 sc1\n"
    "ds_read_b64_tr_b16 v[96:97], v6 offset:4096\n"
    "ds_read_b64_tr_b16 v[98:99], v6 offset:4352\n"
    "v_and_b32 v18, 31, v1\n"
    "v_lshlrev_b32 v8, 3, v18\n"
    "v_and_b32 v18, 15, v1\n"
    "s_waitcnt lgkmcnt(6)\n"
    "v_mfma_f32_32x32x16_bf16 v[112:127], v[148:151], v[100:103], v[112:127]\n"
    "s_getreg_b32 s57, hwreg(HW_REG_IB_STS, 0, 4)\n"
    "s_or_b32 s57, s57, s75\n"
    "s_cmp_gt_u32 s57, 5\n"
    "s_cbranch_scc1 .Lk_s90\n"
    "v_lshlrev_b32 v28, 12, v1\n"
    "s_lshr_b32 s57, s28, 2\n"
    "s_add_u32 s57, s57, 2\n"
    "s_lshl_b32 s57, s57, 15\n"
    "s_add_u32 s46, s98, s57\n"
    "s_addc_u32 s47, s99, 0\n"
    "s_waitcnt vmcnt(5)\n"
    "s_mov_b64 exec, 3\n"
    "global_store_dwordx2 v28, v[4:5], s[46:47] sc1\n"
    "s_mov_b64 exec, -1\n"
    "s_lshl2_add_u32 s58, s28, s27\n"
    "s_cmp_ge_u32 s58, 4\n"
    "s_cbranch_scc1 .Lk_s91\n"
    "s_lshl2_add_u32 s58, s26, s27\n"
    "s_lshl_b32 s58, s58, 7\n"
    "s_add_u32 s58, s58, 18159616\n"
    "s_add_u32 s60, s22, s58\n"
    "s_addc_u32 s61, s23, 0\n"
    "s_mov_b64 exec, 1\n"
    "global_store_dwordx2 v3, v[4:5], s[60:61] sc1\n"
    "s_mov_b64 exec, -1\n"
    ".Lk_s91:\n"
    "s_mov_b32 s75, 64\n"
    ".Lk_s90:\n"
    "ds_read_b64_tr_b16 v[100:101], v6 offset:5120\n"
    "ds_read_b64_tr_b16 v[102:103], v6 offset:5376\n"
    "v_lshlrev_b32 v18, 4, v18\n"
    "v_lshrrev_b32 v22, 5, v1\n"
    "v_lshl_or_b32 v18, v22, 8, v18\n"
    "s_waitcnt lgkmcnt(6)\n"
    "v_mfma_f32_32x32x16_bf16 v[112:127], v[152:155], v[104:107], v[112:127]\n"
    "s_add_u32 m0, s74, 28672\n"
    "s_nop 0\n"
    "global_load_lds_dwordx4 v2, s[92:93] offset:1024 sc1\n"
    "ds_read_b64_tr_b16 v[104:105], v6 offset:6144\n"
    "ds_read_b64_tr_b16 v[106:107], v6 offset:6400\n"
    "v_bfe_u32 v22, v1, 4, 1\n"
    "v_mul_u32_u24 v22, 57344, v22\n"
    "v_add_u32 v9, v22, v18\n"
    "s_waitcnt lgkmcnt(6)\n"
    "v_mfma_f32_32x32x16_bf16 v[112:127], v[156:159], v[108:111], v[112:127]\n"
    "ds_read_b64_tr_b16 v[108:109], v6 offset:7168\n"
    "ds_read_b64_tr_b16 v[110:111], v6 offset:7424\n"
    "v_add_u32 v10, s42, v2\n"
    "v_add_u32 v11, s43, v2\n"
    "s_waitcnt lgkmcnt(6)\n"
    "v_mfma_f32_32x32x16_bf16 v[112:127], v[160:163], v[96:99], v[112:127]\n"
    "s_add_u32 m0, s74, 28672\n"
    "s_nop 0\n"
    "global_load_lds_dwordx4 v2, s[92:93] offset:2048 sc1\n"
    "ds_read_b64_tr_b16 v[96:97], v6 offset:8192\n"
    "ds_read_b64_tr_b16 v[98:99], v6 offset:8448\n"
    "v_add_u32 v12, s44, v2\n"
    "v_min_u32 v18, 6, v1\n"
    "s_waitcnt lgkmcnt(6)\n"
    "v_mfma_f32_32x32x16_bf16 v[112:127], v[164:167], v[100:103], v[112:127]\n"
    "s_getreg_b32 s57, hwreg(HW_REG_IB_STS, 0, 4)\n"
    "s_or_b32 s57, s57, s75\n"
    "s_cmp_gt_u32 s57, 7\n"
    "s_cbranch_scc1 .Lk_s92\n"
    "v_lshlrev_b32 v28, 12, v1\n"
    "s_lshr_b32 s57, s28, 2\n"
    "s_add_u32 s57, s57, 2\n"
    "s_lshl_b32 s57, s57, 15\n"
    "s_add_u32 s46, s98, s57\n"
    "s_addc_u32 s47, s99, 0\n"
    "s_waitcnt vmcnt(7)\n"
    "s_mov_b64 exec, 3\n"
    "global_store_dwordx2 v28, v[4:5], s[46:47] sc1\n"
    "s_mov_b64 exec, -1\n"
    "s_lshl2_add_u32 s58, s28, s27\n"
    "s_cmp_ge_u32 s58, 4\n"
    "s_cbranch_scc1 .Lk_s93\n"
    "s_lshl2_add_u32 s58, s26, s27\n"
    "s_lshl_b32 s58, s58, 7\n"
    "s_add_u32 s58, s58, 18159616\n"
    "s_add_u32 s60, s22, s58\n"
    "s_addc_u32 s61, s23, 0\n"
    "s_mov_b64 exec, 1\n"
    "global_store_dwordx2 v3, v[4:5], s[60:61] sc1\n"
    "s_mov_b64 exec, -1\n"
    ".Lk_s93:\n"
    "s_mov_b32 s75, 64\n"
    ".Lk_s92:\n"
    "ds_read_b64_tr_b16 v[100:101], v6 offset:9216\n"
    "ds_read_b64_tr_b16 v[102:103], v6 offset:9472\n"
    "v_lshlrev_b32 v13, 4, v18\n"
    "v_and_b32 v22, 3, v1\n"
    "s_waitcnt lgkmcnt(6)\n"
    "v_mfma_f32_32x32x16_bf16 v[112:127], v[168:171], v[104:107], v[112:127]\n"
    "s_add_u32 m0, s74, 28672\n"
    "s_mov_b64 exec, 0xffffffff\n"
    "global_load_lds_dwordx4 v2, s[92:93] offset:3072 sc1\n"
    "s_mov_b64 exec, -1\n"
    "ds_read_b64_tr_b16 v[104:105], v6 offset:10240\n"
    "ds_read_b64_tr_b16 v[106:107], v6 offset:10496\n"
    "v_lshrrev_b32 v18, 2, v1\n"
    "v_lshlrev_b32 v18, 6, v18\n"
    "s_waitcnt lgkmcnt(6)\n"
    "v_mfma_f32_32x32x16_bf16 v[112:127], v[172:175], v[108:111], v[112:127]\n"
    "ds_read_b64_tr_b16 v[108:109], v6 offset:11264\n"
    "ds_read_b64_tr_b16 v[110:111], v6 offset:11520\n"
    "v_lshl_or_b32 v18, v22, 3, v18\n"
    "s_lshl_b32 s55, s38, 5\n"
    "s_waitcnt lgkmcnt(6)\n"
    "v_mfma_f32_32x32x16_bf16 v[112:127], v[176:179], v[96:99], v[112:127]\n"
    "s_add_u32 s94, s8, 24576\n"
    "s_addc_u32 s95, s9, 0\n"
    "global_load_dwordx4 v[240:243], v2, s[94:95] offset:0 sc1\n"
    "ds_read_b64_tr_b16 v[96:97], v6 offset:12288\n"
    "ds_read_b64_tr_b16 v[98:99], v6 offset:12544\n"
    "s_add_u32 s55, s55, s42\n"
    "v_add_u32 v31, s55, v18\n"
    "s_waitcnt lgkmcnt(6)\n"
    "v_mfma_f32_32x32x16_bf16 v[112:127], v[180:183], v[100:103], v[112:127]\n"
    "global_load_dwordx4 v[244:247], v2, s[94:95] offset:1024 sc1\n"
    "s_cmp_lg_u32 s75, 0\n"
    "s_cbranch_scc1 .Lk_s94\n"
    "v_lshlrev_b32 v28, 12, v1\n"
    "s_lshr_b32 s57, s28, 2\n"
    "s_add_u32 s57, s57, 2\n"
    "s_lshl_b32 s57, s57, 15\n"
    "s_add_u32 s46, s98, s57\n"
    "s_addc_u32 s47, s99, 0\n"
    "s_waitcnt vmcnt(10)\n"
    "s_mov_b64 exec, 3\n"
    "global_store_dwordx2 v28, v[4:5], s[46:47] sc1\n"
    "s_mov_b64 exec, -1\n"
    "s_lshl2_add_u32 s58, s28, s27\n"
    "s_cmp_ge_u32 s58, 4\n"
    "s_cbranch_scc1 .Lk_s95\n"
    "s_lshl2_add_u32 s58, s26, s27\n"
    "s_lshl_b32 s58, s58, 7\n"
    "s_add_u32 s58, s58, 18159616\n"
    "s_add_u32 s60, s22, s58\n"
    "s_addc_u32 s61, s23, 0\n"
    "s_mov_b64 exec, 1\n"
    "global_store_dwordx2 v3, v[4:5], s[60:61] sc1\n"
    "s_mov_b64 exec, -1\n"
    ".Lk_s95:\n"
    "s_mov_b32 s75, 64\n"
    ".Lk_s94:\n"
    "ds_read_b64_tr_b16 v[100:101], v6 offset:13312\n"
    "ds_read_b64_tr_b16 v[102:103], v6 offset:13568\n"
    "v_min_u32 v18, 13, v1\n"
    "s_mul_i32 s55, s28, 14\n"
    "s_waitcnt lgkmcnt(6)\n"
    "v_mfma_f32_32x32x16_bf16 v[112:127], v[184:187], v[104:107], v[112:127]\n"
    "ds_read_b64_tr_b16 v[104:105], v6 offset:14336\n"
    "ds_read_b64_tr_b16 v[106:107], v6 offset:14592\n"
    "s_lshl_b32 s56, s37, 7\n"
    "s_add_u32 s55, s55, s56\n"
    "s_waitcnt lgkmcnt(6)\n"
    "v_mfma_f32_32x32x16_bf16 v[112:127], v[188:191], v[108:111], v[112:127]\n"
    "global_load_dwordx4 v[248:251], v2, s[94:95] offset:2048 sc1\n"
    "ds_read_b64_tr_b16 v[108:109], v6 offset:15360\n"
    "ds_read_b64_tr_b16 v[110:111], v6 offset:15616\n"
    "v_add_lshl_u32 v30, v18, s55, 4\n"
    "s_mov_b32 s65, 0x01000100\n"
    "s_waitcnt lgkmcnt(6)\n"
    "v_mfma_f32_32x32x16_bf16 v[112:127], v[192:195], v[96:99], v[112:127]\n"
    "global_load_dwordx4 v[252:255], v2, s[94:95] offset:3072 sc1\n"
    "ds_read_b64_tr_b16 v[96:97], v6 offset:16384\n"
    "ds_read_b64_tr_b16 v[98:99], v6 offset:16640\n"
    "s_waitcnt lgkmcnt(6)\n"
    "v_mfma_f32_32x32x16_bf16 v[112:127], v[196:199], v[100:103], v[112:127]\n"
    "s_lshl_b32 s57, s40, 8\n"
    "s_add_u32 s57, s57, 18126848\n"
    "s_add_u32 s10, s22, s57\n"
    "s_addc_u32 s11, s23, 0\n"
    "global_load_dwordx2 v[46:47], v8, s[10:11] sc1\n"
    "ds_read_b64_tr_b16 v[100:101], v6 offset:17408\n"
    "ds_read_b64_tr_b16 v[102:103], v6 offset:17664\n"
    "s_waitcnt lgkmcnt(6)\n"
    "v_mfma_f32_32x32x16_bf16 v[112:127], v[200:203], v[104:107], v[112:127]\n"
    "s_add_u32 s94, s88, 0\n"
    "s_addc_u32 s95, s89, 0\n"
    "global_load_dwordx4 v[48:51], v2, s[94:95] offset:0 sc1\n"
    "ds_read_b64_tr_b16 v[104:105], v6 offset:18432\n"
    "ds_read_b64_tr_b16 v[106:107], v6 offset:18688\n"
    "s_waitcnt lgkmcnt(6)\n"
    "v_mfma_f32_32x32x16_bf16 v[112:127], v[204:207], v[108:111], v[112:127]\n"
    "ds_read_b64_tr_b16 v[108:109], v6 offset:19456\n"
    "ds_read_b64_tr_b16 v[110:111], v6 offset:19712\n"
    "s_waitcnt lgkmcnt(6)\n"
    "v_mfma_f32_32x32x16_bf16 v[112:127], v[208:211], v[96:99], v[112:127]\n"
    "global_load_dwordx4 v[52:55], v2, s[94:95] offset:1024 sc1\n"
    "ds_read_b64_tr_b16 v[96:97], v6 offset:20480\n"
    "ds_read_b64_tr_b16 v[98:99], v6 offset:20736\n"
    "s_waitcnt lgkmcnt(6)\n"
    "v_mfma_f32_32x32x16_bf16 v[112:127], v[212:215], v[100:103], v[112:127]\n"
    "s_add_u32 s48, s78, 65536\n"
    "s_addc_u32 s49, s79, 0\n"
    "global_load_dwordx2 v[40:41], v7, s[48:49] sc1\n"
    "ds_read_b64_tr_b16 v[100:101], v6 offset:21504\n"
    "ds_read_b64_tr_b16 v[102:103], v6 offset:21760\n"
    "s_waitcnt lgkmcnt(6)\n"
    "v_mfma_f32_32x32x16_bf16 v[112:127], v[216:219], v[104:107], v[112:127]\n"
    "global_load_dwordx4 v[56:59], v2, s[94:95] offset:2048 sc1\n"
    "ds_read_b64_tr_b16 v[104:105], v6 offset:22528\n"
    "ds_read_b64_tr_b16 v[106:107], v6 offset:22784\n"
    "s_waitcnt lgkmcnt(6)\n"
    "v_mfma_f32_32x32x16_bf16 v[112:127], v[220:223], v[108:111], v[112:127]\n"
    "s_add_u32 s50, s82, 0\n"
    "s_addc_u32 s51, s83, 0\n"
    "global_load_dwordx2 v[42:43], v8, s[50:51] sc1\n"
    "ds_read_b64_tr_b16 v[108:109], v6 offset:23552\n"
    "ds_read_b64_tr_b16 v[110:111], v6 offset:23808\n"
    "s_waitcnt vmcnt(17) lgkmcnt(6)\n"
    "v_mfma_f32_32x32x16_bf16 v[112:127], v[224:227], v[96:99], v[112:127]\n"
    "global_load_dwordx4 v[60:63], v2, s[94:95] offset:3072 sc1\n"
    "ds_read_b64_tr_b16 v[96:97], v6 offset:24576\n"
    "ds_read_b64_tr_b16 v[98:99], v6 offset:24832\n"
    "s_waitcnt vmcnt(17) lgkmcnt(6)\n"
    "v_mfma_f32_32x32x16_bf16 v[112:127], v[228:231], v[100:103], v[112:127]\n"
    "ds_read_b64_tr_b16 v[100:101], v6 offset:25600\n"
    "ds_read_b64_tr_b16 v[102:103], v6 offset:25856\n"
    "s_waitcnt vmcnt(16) lgkmcnt(6)\n"
    "v_mfma_f32_32x32x16_bf16 v[112:127], v[232:235], v[104:107], v[112:127]\n"
    "s_add_u32 s94, s88, 4096\n"
    "s_addc_u32 s95, s89, 0\n"
    "global_load_dwordx4 v[64:67], v2, s[94:95] offset:0 sc1\n"
    "ds_read_b64_tr_b16 v[104:105], v6 offset:26624\n"
    "ds_read_b64_tr_b16 v[106:107], v6 offset:26880\n"
    "s_waitcnt vmcnt(16) lgkmcnt(6)\n"
    "v_mfma_f32_32x32x16_bf16 v[112:127], v[236:239], v[108:111], v[112:127]\n"
    "s_waitcnt vmcnt(12)\n"
    "s_barrier\n"
    "ds_read_b64_tr_b16 v[108:109], v6 offset:27648\n"
    "ds_read_b64_tr_b16 v[110:111], v6 offset:27904\n"
    "s_waitcnt vmcnt(11) lgkmcnt(6)\n"
    "v_mfma_f32_32x32x16_bf16 v[112:127], v[240:243], v[96:99], v[112:127]\n"
    "global_load_dwordx4 v[68:71], v2, s[94:95] offset:1024 sc1\n"
    "ds_read_b64_tr_b16 v[96:97], v6 offset:28672\n"
    "ds_read_b64_tr_b16 v[98:99], v6 offset:28928\n"
    "s_waitcnt vmcnt(11) lgkmcnt(6)\n"
    "v_mfma_f32_32x32x16_bf16 v[112:127], v[244:247], v[100:103], v[112:127]\n"
    "ds_read_b64_tr_b16 v[100:101], v6 offset:29696\n"
    "ds_read_b64_tr_b16 v[102:103], v6 offset:29952\n"
    "s_waitcnt vmcnt(10) lgkmcnt(6)\n"
    "v_mfma_f32_32x32x16_bf16 v[112:127], v[248:251], v[104:107], v[112:127]\n"
    "global_load_dwordx4 v[72:75], v2, s[94:95] offset:2048 sc1\n"
    "ds_read_b64_tr_b16 v[104:105], v6 offset:30720\n"
    "ds_read_b64_tr_b16 v[106:107], v6 offset:30976\n"
    "s_waitcnt vmcnt(10) lgkmcnt(6)\n"
    "v_mfma_f32_32x32x16_bf16 v[112:127], v[252:255], v[108:111], v[112:127]\n"
    "ds_read_b64_tr_b16 v[108:109], v6 offset:31744\n"
    "ds_read_b64_tr_b16 v[110:111], v6 offset:32000\n"
    "s_waitcnt lgkmcnt(6)\n"
    "v_mfma_f32_32x32x16_bf16 v[128:143], v[144:147], v[96:99], 0\n"
    "global_load_dwordx4 v[76:79], v2, s[94:95] offset:3072 sc1\n"
    "ds_read_b64_tr_b16 v[96:97], v6 offset:32768\n"
    "ds_read_b64_tr_b16 v[98:99], v6 offset:33024\n"
    "s_waitcnt lgkmcnt(6)\n"
    "v_mfma_f32_32x32x16_bf16 v[128:143], v[148:151], v[100:103], v[128:143]\n"
    "s_waitcnt vmcnt(10)\n"
    "v_cmp_ne_u32 vcc, s34, v47\n"
    "s_cbranch_vccnz .Lk_r96\n"
    ".Lk_c97:\n"
    "v_cmp_gt_u32 vcc, 32, v1\n"
    "v_mov_b32 v47, 0\n"
    "v_mov_b32 v45, 0\n"
    "v_cndmask_b32 v46, v3, v46, vcc\n"
    "ds_read_b64_tr_b16 v[100:101], v6 offset:33792\n"
    "ds_read_b64_tr_b16 v[102:103], v6 offset:34048\n"
    "s_waitcnt lgkmcnt(6)\n"
    "v_mfma_f32_32x32x16_bf16 v[128:143], v[152:155], v[104:107], v[128:143]\n"
    "s_waitcnt vmcnt(7)\n"
    "v_cmp_ne_u64 vcc, s[34:35], v[40:41]\n"
    "s_cbranch_vccnz .Lk_r99\n"
    ".Lk_c100:\n"
    "s_add_u32 m0, s74, 98304\n"
    "s_add_u32 s92, s80, 917504\n"
    "s_addc_u32 s93, s81, 0\n"
    "global_load_lds_dwordx4 v2, s[92:93] offset:0 sc1\n"
    "ds_read_b64_tr_b16 v[104:105], v6 offset:34816\n"
    "ds_read_b64_tr_b16 v[106:107], v6 offset:35072\n"
    "s_waitcnt lgkmcnt(6)\n"
    "v_mfma_f32_32x32x16_bf16 v[128:143], v[156:159], v[108:111], v[128:143]\n"
    "s_add_u32 m0, s74, 98304\n"
    "s_nop 0\n"
    "global_load_lds_dwordx4 v2, s[92:93] offset:1024 sc1\n"
    "ds_read_b64_tr_b16 v[108:109], v6 offset:35840\n"
    "ds_read_b64_tr_b16 v[110:111], v6 offset:36096\n"
    "s_waitcnt lgkmcnt(6)\n"
    "v_mfma_f32_32x32x16_bf16 v[128:143], v[160:163], v[96:99], v[128:143]\n"
    "ds_read_b64_tr_b16 v[96:97], v6 offset:36864\n"
    "ds_read_b64_tr_b16 v[98:99], v6 offset:37120\n"
    "s_waitcnt lgkmcnt(6)\n"
    "v_mfma_f32_32x32x16_bf16 v[128:143], v[164:167], v[100:103], v[128:143]\n"
    "s_add_u32 m0, s74, 98304\n"
    "s_nop 0\n"
    "global_load_lds_dwordx4 v2, s[92:93] offset:2048 sc1\n"
    "ds_read_b64_tr_b16 v[100:101], v6 offset:37888\n"
    "ds_read_b64_tr_b16 v[102:103], v6 offset:38144\n"
    "s_waitcnt lgkmcnt(6)\n"
    "v_mfma_f32_32x32x16_bf16 v[128:143], v[168:171], v[104:107], v[128:143]\n"
    "s_add_u32 m0, s74, 98304\n"
    "s_mov_b64 exec, 0xffffffff\n"
    "global_load_lds_dwordx4 v2, s[92:93] offset:3072 sc1\n"
    "s_mov_b64 exec, -1\n"
    "ds_read_b64_tr_b16 v[104:105], v6 offset:38912\n"
    "ds_read_b64_tr_b16 v[106:107], v6 offset:39168\n"
    "s_waitcnt lgkmcnt(6)\n"
    "v_mfma_f32_32x32x16_bf16 v[128:143], v[172:175], v[108:111], v[128:143]\n"
    "s_add_u32 s48, s78, 98304\n"
    "s_addc_u32 s49, s79, 0\n"
    "global_load_dwordx2 v[40:41], v7, s[48:49] sc1\n"
    "ds_read_b64_tr_b16 v[108:109], v6 offset:39936\n"
    "ds_read_b64_tr_b16 v[110:111], v6 offset:40192\n"
    "s_waitcnt lgkmcnt(6)\n"
    "v_mfma_f32_32x32x16_bf16 v[128:143], v[176:179], v[96:99], v[128:143]\n"
    "s_add_u32 s94, s88, 8192\n"
    "s_addc_u32 s95, s89, 0\n"
    "global_load_dwordx4 v[80:83], v2, s[94:95] offset:0 sc1\n"
    "ds_read_b64_tr_b16 v[96:97], v6 offset:40960\n"
    "ds_read_b64_tr_b16 v[98:99], v6 offset:41216\n"
    "s_waitcnt lgkmcnt(6)\n"
    "v_mfma_f32_32x32x16_bf16 v[128:143], v[180:183], v[100:103], v[128:143]\n"
    "ds_read_b64_tr_b16 v[100:101], v6 offset:41984\n"
    "ds_read_b64_tr_b16 v[102:103], v6 offset:42240\n"
    "s_waitcnt vmcnt(11)\n"
    "v_cmp_ne_u32 vcc, s34, v43\n"
    "s_cbranch_vccnz .Lk_r102\n"
    ".Lk_c103:\n"
    "v_perm_b32 v44, v42, v42, s65\n"
    "v_and_b32 v34, 0xffff0000, v42\n"
    "v_xor_b32 v44, 0x80008000, v44\n"
    "v_rcp_f32 v33, v34\n"
    "v_mul_f32 v34, v34, v34\n"
    "v_mfma_f32_32x32x8_bf16 v[112:127], v[46:47], v[44:45], v[112:127]\n"
    "s_waitcnt lgkmcnt(6)\n"
    "v_mfma_f32_32x32x16_bf16 v[128:143], v[184:187], v[104:107], v[128:143]\n"
    "global_load_dwordx4 v[84:87], v2, s[94:95] offset:1024 sc1\n"
    "ds_read_b64_tr_b16 v[104:105], v6 offset:43008\n"
    "ds_read_b64_tr_b16 v[106:107], v6 offset:43264\n"
    "v_mul_f32 v32, 0xc0135761, v33\n"
    "v_mul_f32 v33, v33, v33\n"
    "v_mul_f32 v33, v33, v32\n"
    "v_mul_f32 v33, 0x3d372713, v33\n"
    "s_waitcnt lgkmcnt(6)\n"
    "v_mfma_f32_32x32x16_bf16 v[128:143], v[188:191], v[108:111], v[128:143]\n"
    "ds_read_b64_tr_b16 v[108:109], v6 offset:44032\n"
    "ds_read_b64_tr_b16 v[110:111], v6 offset:44288\n"
    "s_waitcnt lgkmcnt(6)\n"
    "v_mfma_f32_32x32x16_bf16 v[128:143], v[192:195], v[96:99], v[128:143]\n"
    "global_load_dwordx4 v[88:91], v2, s[94:95] offset:2048 sc1\n"
    "ds_read_b64_tr_b16 v[96:97], v6 offset:45056\n"
    "ds_read_b64_tr_b16 v[98:99], v6 offset:45312\n"
    "v_mul_f32 v24, v112, v112\n"
    "v_mul_f32 v25, v113, v113\n"
    "v_fma_f32 v24, v24, v33, v32\n"
    "v_fma_f32 v25, v25, v33, v32\n"
    "v_mul_f32 v24, v112, v24\n"
    "v_mul_f32 v25, v113, v25\n"
    "s_waitcnt lgkmcnt(6)\n"
    "v_mfma_f32_32x32x16_bf16 v[128:143], v[196:199], v[100:103], v[128:143]\n"
    "s_add_u32 s50, s82, 4096\n"
    "s_addc_u32 s51, s83, 0\n"
    "global_load_dwordx2 v[42:43], v8, s[50:51] sc1\n"
    "ds_read_b64_tr_b16 v[100:101], v6 offset:46080\n"
    "ds_read_b64_tr_b16 v[102:103], v6 offset:46336\n"
    "v_exp_f32 v24, v24\n"
    "v_exp_f32 v25, v25\n"
    "v_mul_f32 v120, v112, v120\n"
    "v_mul_f32 v121, v113, v121\n"
    "s_waitcnt lgkmcnt(6)\n"
    "v_mfma_f32_32x32x16_bf16 v[128:143], v[200:203], v[104:107], v[128:143]\n"
    "global_load_dwordx4 v[92:95], v2, s[94:95] offset:3072 sc1\n"
    "ds_read_b64_tr_b16 v[104:105], v6 offset:47104\n"
    "ds_read_b64_tr_b16 v[106:107], v6 offset:47360\n"
    "v_fma_f32 v24, v24, v34, v34\n"
    "v_fma_f32 v25, v25, v34, v34\n"
    "v_rcp_f32 v24, v24\n"
    "v_rcp_f32 v25, v25\n"
    "s_waitcnt lgkmcnt(6)\n"
    "v_mfma_f32_32x32x16_bf16 v[128:143], v[204:207], v[108:111], v[128:143]\n"
    "ds_read_b64_tr_b16 v[108:109], v6 offset:48128\n"
    "ds_read_b64_tr_b16 v[110:111], v6 offset:48384\n"
    "v_mul_f32 v26, v114, v114\n"
    "v_mul_f32 v27, v115, v115\n"
    "v_mul_f32 v120, v120, v24\n"
    "v_mul_f32 v121, v121, v25\n"
    "v_cvt_pk_bf16_f32 v36, v120, v121\n"
    "v_fma_f32 v26, v26, v33, v32\n"
    "s_waitcnt lgkmcnt(6)\n"
    "v_mfma_f32_32x32x16_bf16 v[128:143], v[208:211], v[96:99], v[128:143]\n"
    "s_add_u32 m0, s42, 0\n"
    "s_add_u32 s94, s88, 12288\n"
    "s_addc_u32 s95, s89, 0\n"
    "global_load_lds_dwordx4 v2, s[94:95] offset:0 sc1\n"
    "ds_read_b64_tr_b16 v[96:97], v6 offset:49152\n"
    "ds_read_b64_tr_b16 v[98:99], v6 offset:49408\n"
    "v_fma_f32 v27, v27, v33, v32\n"
    "v_mul_f32 v26, v114, v26\n"
    "v_mul_f32 v27, v115, v27\n"
    "v_exp_f32 v26, v26\n"
    "s_waitcnt lgkmcnt(6)\n"
    "v_mfma_f32_32x32x16_bf16 v[128:143], v[212:215], v[100:103], v[128:143]\n"
    "ds_read_b64_tr_b16 v[100:101], v6 offset:50176\n"
    "ds_read_b64_tr_b16 v[102:103], v6 offset:50432\n"
    "v_exp_f32 v27, v27\n"
    "v_mul_f32 v122, v114, v122\n"
    "v_mul_f32 v123, v115, v123\n"
    "v_fma_f32 v26, v26, v34, v34\n"
    "s_waitcnt lgkmcnt(6)\n"
    "v_mfma_f32_32x32x16_bf16 v[128:143], v[216:219], v[104:107], v[128:143]\n"
    "s_add_u32 m0, s42, 0\n"
    "s_nop 0\n"
    "global_load_lds_dwordx4 v2, s[94:95] offset:1024 sc1\n"
    "ds_read_b64_tr_b16 v[104:105], v6 offset:51200\n"
    "ds_read_b64_tr_b16 v[106:107], v6 offset:51456\n"
    "v_fma_f32 v27, v27, v34, v34\n"
    "v_rcp_f32 v26, v26\n"
    "v_rcp_f32 v27, v27\n"
    "s_waitcnt lgkmcnt(6)\n"
    "v_mfma_f32_32x32x16_bf16 v[128:143], v[220:223], v[108:111], v[128:143]\n"
    "ds_read_b64_tr_b16 v[108:109], v6 offset:52224\n"
    "ds_read_b64_tr_b16 v[110:111], v6 offset:52480\n"
    "v_mul_f32 v24, v116, v116\n"
    "v_mul_f32 v25, v117, v117\n"
    "v_mul_f32 v122, v122, v26\n"
    "v_mul_f32 v123, v123, v27\n"
    "v_cvt_pk_bf16_f32 v37, v122, v123\n"
    "s_waitcnt lgkmcnt(6)\n"
    "v_mfma_f32_32x32x16_bf16 v[128:143], v[224:227], v[96:99], v[128:143]\n"
    "s_add_u32 m0, s42, 0\n"
    "s_nop 0\n"
    "global_load_lds_dwordx4 v2, s[94:95] offset:2048 sc1\n"
    "ds_read_b64_tr_b16 v[96:97], v6 offset:53248\n"
    "ds_read_b64_tr_b16 v[98:99], v6 offset:53504\n"
    "v_fma_f32 v24, v24, v33, v32\n"
    "v_fma_f32 v25, v25, v33, v32\n"
    "v_mul_f32 v24, v116, v24\n"
    "v_mul_f32 v25, v117, v25\n"
    "v_exp_f32 v24, v24\n"
    "s_waitcnt lgkmcnt(6)\n"
    "v_mfma_f32_32x32x16_bf16 v[128:143], v[228:231], v[100:103], v[128:143]\n"
    "ds_read_b64_tr_b16 v[100:101], v6 offset:54272\n"
    "ds_read_b64_tr_b16 v[102:103], v6 offset:54528\n"
    "v_exp_f32 v25, v25\n"
    "v_mul_f32 v124, v116, v124\n"
    "v_mul_f32 v125, v117, v125\n"
    "v_fma_f32 v24, v24, v34, v34\n"
    "s_waitcnt lgkmcnt(6)\n"
    "v_mfma_f32_32x32x16_bf16 v[128:143], v[232:235], v[104:107], v[128:143]\n"
    "s_add_u32 m0, s42, 0\n"
    "s_nop 0\n"
    "global_load_lds_dwordx4 v2, s[94:95] offset:3072 sc1\n"
    "s_waitcnt vmcnt(9)\n"
    "v_cmp_ne_u64 vcc, s[34:35], v[40:41]\n"
    "s_cbranch_vccnz .Lk_r105\n"
    ".Lk_c106:\n"
    "ds_read_b64_tr_b16 v[104:105], v6 offset:55296\n"
    "ds_read_b64_tr_b16 v[106:107], v6 offset:55552\n"
    "v_fma_f32 v25, v25, v34, v34\n"
    "v_rcp_f32 v24, v24\n"
    "v_rcp_f32 v25, v25\n"
    "s_waitcnt lgkmcnt(6)\n"
    "v_mfma_f32_32x32x16_bf16 v[128:143], v[236:239], v[108:111], v[128:143]\n"
    "s_barrier\n"
    "ds_read_b64_tr_b16 v[108:109], v6 offset:56320\n"
    "ds_read_b64_tr_b16 v[110:111], v6 offset:56576\n"
    "v_mul_f32 v26, v118, v118\n"
    "v_mul_f32 v27, v119, v119\n"
    "v_mul_f32 v124, v124, v24\n"
    "v_mul_f32 v125, v125, v25\n"
    "v_cvt_pk_bf16_f32 v38, v124, v125\n"
    "s_waitcnt lgkmcnt(6)\n"
    "v_mfma_f32_32x32x16_bf16 v[128:143], v[240:243], v[96:99], v[128:143]\n"
    "s_add_u32 m0, s74, 0\n"
    "s_add_u32 s92, s80, 1376256\n"
    "s_addc_u32 s93, s81, 0\n"
    "global_load_lds_dwordx4 v2, s[92:93] offset:0 sc1\n"
    "ds_read_b64_tr_b16 v[96:97], v19 offset:0\n"
    "ds_read_b64_tr_b16 v[98:99], v19 offset:256\n"
    "v_fma_f32 v26, v26, v33, v32\n"
    "v_fma_f32 v27, v27, v33, v32\n"
    "v_mul_f32 v26, v118, v26\n"
    "v_mul_f32 v27, v119, v27\n"
    "v_exp_f32 v26, v26\n"
    "s_waitcnt lgkmcnt(6)\n"
    "v_mfma_f32_32x32x16_bf16 v[128:143], v[244:247], v[100:103], v[128:143]\n"
    "ds_read_b64_tr_b16 v[100:101], v19 offset:1024\n"
    "ds_read_b64_tr_b16 v[102:103], v19 offset:1280\n"
    "v_exp_f32 v27, v27\n"
    "v_mul_f32 v126, v118, v126\n"
    "v_mul_f32 v127, v119, v127\n"
    "v_fma_f32 v26, v26, v34, v34\n"
    "s_waitcnt lgkmcnt(6)\n"
    "v_mfma_f32_32x32x16_bf16 v[128:143], v[248:251], v[104:107], v[128:143]\n"
    "s_add_u32 m0, s74, 0\n"
    "s_nop 0\n"
    "global_load_lds_dwordx4 v2, s[92:93] offset:1024 sc1\n"
    "ds_read_b64_tr_b16 v[104:105], v19 offset:2048\n"
    "ds_read_b64_tr_b16 v[106:107], v19 offset:2304\n"
    "v_fma_f32 v27, v27, v34, v34\n"
    "v_rcp_f32 v26, v26\n"
    "v_rcp_f32 v27, v27\n"
    "s_waitcnt lgkmcnt(6)\n"
    "v_mfma_f32_32x32x16_bf16 v[128:143], v[252:255], v[108:111], v[128:143]\n"
    "ds_read_b64_tr_b16 v[108:109], v19 offset:3072\n"
    "ds_read_b64_tr_b16 v[110:111], v19 offset:3328\n"
    "v_mul_f32 v126, v126, v26\n"
    "v_mul_f32 v127, v127, v27\n"
    "v_cvt_pk_bf16_f32 v39, v126, v127\n"
    "s_add_u32 s46, s84, 0\n"
    "s_addc_u32 s47, s85, 0\n"
    "global_store_dwordx4 v9, v[36:39], s[46:47] sc1\n"
    "s_mov_b32 s75, 0\n"
    "s_waitcnt lgkmcnt(6)\n"
    "v_mfma_f32_32x32x16_bf16 v[112:127], v[144:147], v[96:99], 0\n"
    "s_add_u32 m0, s74, 0\n"
    "s_nop 0\n"
    "global_load_lds_dwordx4 v2, s[92:93] offset:2048 sc1\n"
    "ds_read_b64_tr_b16 v[96:97], v19 offset:4096\n"
    "ds_read_b64_tr_b16 v[98:99], v19 offset:4352\n"
    "s_waitcnt lgkmcnt(6)\n"
    "v_mfma_f32_32x32x16_bf16 v[112:127], v[148:151], v[100:103], v[112:127]\n"
    "ds_read_b64_tr_b16 v[100:101], v19 offset:5120\n"
    "ds_read_b64_tr_b16 v[102:103], v19 offset:5376\n"
    "s_waitcnt vmcnt(9)\n"
    "v_cmp_ne_u32 vcc, s34, v43\n"
    "s_cbranch_vccnz .Lk_r108\n"
    ".Lk_c109:\n"
    "v_perm_b32 v44, v42, v42, s65\n"
    "v_and_b32 v34, 0xffff0000, v42\n"
    "v_xor_b32 v44, 0x80008000, v44\n"
    "v_rcp_f32 v33, v34\n"
    "v_mul_f32 v34, v34, v34\n"
    "v_mfma_f32_32x32x8_bf16 v[128:143], v[46:47], v[44:45], v[128:143]\n"
    "s_waitcnt lgkmcnt(6)\n"
    "v_mfma_f32_32x32x16_bf16 v[112:127], v[152:155], v[104:107], v[112:127]\n"
    "s_add_u32 m0, s74, 0\n"
    "s_mov_b64 exec, 0xffffffff\n"
    "global_load_lds_dwordx4 v2, s[92:93] offset:3072 sc1\n"
    "s_mov_b64 exec, -1\n"
    "ds_read_b64_tr_b16 v[104:105], v19 offset:6144\n"
    "ds_read_b64_tr_b16 v[106:107], v19 offset:6400\n"
    "v_mul_f32 v32, 0xc0135761, v33\n"
    "v_mul_f32 v33, v33, v33\n"
    "v_mul_f32 v33, v33, v32\n"
    "v_mul_f32 v33, 0x3d372713, v33\n"
    "s_waitcnt lgkmcnt(6)\n"
    "v_mfma_f32_32x32x16_bf16 v[112:127], v[156:159], v[108:111], v[112:127]\n"
    "s_barrier\n"
    "ds_read_b64_tr_b16 v[108:109], v19 offset:7168\n"
    "ds_read_b64_tr_b16 v[110:111], v19 offset:7424\n"
    "s_waitcnt lgkmcnt(6)\n"
    "v_mfma_f32_32x32x16_bf16 v[112:127], v[160:163], v[96:99], v[112:127]\n"
    "s_add_u32 m0, s42, 4096\n"
    "s_add_u32 s94, s88, 16384\n"
    "s_addc_u32 s95, s89, 0\n"
    "global_load_lds_dwordx4 v2, s[94:95] offset:0 sc1\n"
    "ds_read_b64_tr_b16 v[96:97], v19 offset:8192\n"
    "ds_read_b64_tr_b16 v[98:99], v19 offset:8448\n"
    "v_mul_f32 v24, v128, v128\n"
    "v_mul_f32 v25, v129, v129\n"
    "v_fma_f32 v24, v24, v33, v32\n"
    "v_fma_f32 v25, v25, v33, v32\n"
    "v_mul_f32 v24, v128, v24\n"
    "s_waitcnt lgkmcnt(6)\n"
    "v_mfma_f32_32x32x16_bf16 v[112:127], v[164:167], v[100:103], v[112:127]\n"
    "ds_read_b64_tr_b16 v[100:101], v19 offset:9216\n"
    "ds_read_b64_tr_b16 v[102:103], v19 offset:9472\n"
    "v_mul_f32 v25, v129, v25\n"
    "v_exp_f32 v24, v24\n"
    "v_exp_f32 v25, v25\n"
    "s_waitcnt lgkmcnt(6)\n"
    "v_mfma_f32_32x32x16_bf16 v[112:127], v[168:171], v[104:107], v[112:127]\n"
    "s_add_u32 m0, s42, 4096\n"
    "s_nop 0\n"
    "global_load_lds_dwordx4 v2, s[94:95] offset:1024 sc1\n"
    "ds_read_b64_tr_b16 v[104:105], v19 offset:10240\n"
    "ds_read_b64_tr_b16 v[106:107], v19 offset:10496\n"
    "v_mul_f32 v136, v128, v136\n"
    "v_mul_f32 v137, v129, v137\n"
    "v_fma_f32 v24, v24, v34, v34\n"
    "v_fma_f32 v25, v25, v34, v34\n"
    "v_rcp_f32 v24, v24\n"
    "s_waitcnt lgkmcnt(6)\n"
    "v_mfma_f32_32x32x16_bf16 v[112:127], v[172:175], v[108:111], v[112:127]\n"
    "ds_read_b64_tr_b16 v[108:109], v19 offset:11264\n"
    "ds_read_b64_tr_b16 v[110:111], v19 offset:11520\n"
    "v_rcp_f32 v25, v25\n"
    "v_mul_f32 v26, v130, v130\n"
    "v_mul_f32 v27, v131, v131\n"
    "v_mul_f32 v136, v136, v24\n"
    "s_waitcnt lgkmcnt(6)\n"
    "v_mfma_f32_32x32x16_bf16 v[112:127], v[176:179], v[96:99], v[112:127]\n"
    "s_add_u32 m0, s42, 4096\n"
    "s_nop 0\n"
    "global_load_lds_dwordx4 v2, s[94:95] offset:2048 sc1\n"
    "s_getreg_b32 s57, hwreg(HW_REG_IB_STS, 0, 4)\n"
    "s_or_b32 s57, s57, s75\n"
    "s_cmp_gt_u32 s57, 5\n"
    "s_cbranch_scc1 .Lk_s111\n"
    "s_add_u32 s46, s86, 0\n"
    "s_addc_u32 s47, s87, 0\n"
    "s_waitcnt vmcnt(5)\n"
    "s_mov_b64 exec, 1\n"
    "global_store_dwordx2 v3, v[4:5], s[46:47] sc1\n"
    "s_mov_b64 exec, -1\n"
    "s_mov_b32 s75, 64\n"
    ".Lk_s111:\n"
    "ds_read_b64_tr_b16 v[96:97], v19 offset:12288\n"
    "ds_read_b64_tr_b16 v[98:99], v19 offset:12544\n"
    "v_mul_f32 v137, v137, v25\n"
    "v_cvt_pk_bf16_f32 v36, v136, v137\n"
    "v_fma_f32 v26, v26, v33, v32\n"
    "v_fma_f32 v27, v27, v33, v32\n"
    "v_mul_f32 v26, v130, v26\n"
    "s_waitcnt lgkmcnt(6)\n"
    "v_mfma_f32_32x32x16_bf16 v[112:127], v[180:183], v[100:103], v[112:127]\n"
    "ds_read_b64_tr_b16 v[100:101], v19 offset:13312\n"
    "ds_read_b64_tr_b16 v[102:103], v19 offset:13568\n"
    "v_mul_f32 v27, v131, v27\n"
    "v_exp_f32 v26, v26\n"
    "v_exp_f32 v27, v27\n"
    "s_waitcnt lgkmcnt(6)\n"
    "v_mfma_f32_32x32x16_bf16 v[112:127], v[184:187], v[104:107], v[112:127]\n"
    "s_add_u32 m0, s42, 4096\n"
    "s_nop 0\n"
    "global_load_lds_dwordx4 v2, s[94:95] offset:3072 sc1\n"
    "s_getreg_b32 s57, hwreg(HW_REG_IB_STS, 0, 4)\n"
    "s_or_b32 s57, s57, s75\n"
    "s_cmp_gt_u32 s57, 6\n"
    "s_cbranch_scc1 .Lk_s112\n"
    "s_add_u32 s46, s86, 0\n"
    "s_addc_u32 s47, s87, 0\n"
    "s_waitcnt vmcnt(6)\n"
    "s_mov_b64 exec, 1\n"
    "global_store_dwordx2 v3, v[4:5], s[46:47] sc1\n"
    "s_mov_b64 exec, -1\n"
    "s_mov_b32 s75, 64\n"
    ".Lk_s112:\n"
    "ds_read_b64_tr_b16 v[104:105], v19 offset:14336\n"
    "ds_read_b64_tr_b16 v[106:107], v19 offset:14592\n"
    "v_mul_f32 v138, v130, v138\n"
    "v_mul_f32 v139, v131, v139\n"
    "v_fma_f32 v26, v26, v34, v34\n"
    "v_fma_f32 v27, v27, v34, v34\n"
    "v_rcp_f32 v26, v26\n"
    "s_waitcnt lgkmcnt(6)\n"
    "v_mfma_f32_32x32x16_bf16 v[112:127], v[188:191], v[108:111], v[112:127]\n"
    "s_add_u32 s50, s82, 8192\n"
    "s_addc_u32 s51, s83, 0\n"
    "global_load_dwordx2 v[42:43], v8, s[50:51] sc1\n"
    "ds_read_b64_tr_b16 v[108:109], v19 offset:15360\n"
    "ds_read_b64_tr_b16 v[110:111], v19 offset:15616\n"
    "v_rcp_f32 v27, v27\n"
    "v_mul_f32 v24, v132, v132\n"
    "v_mul_f32 v25, v133, v133\n"
    "v_mul_f32 v138, v138, v26\n"
    "s_waitcnt lgkmcnt(6)\n"
    "v_mfma_f32_32x32x16_bf16 v[112:127], v[192:195], v[96:99], v[112:127]\n"
    "s_add_u32 m0, s43, 0\n"
    "s_add_u32 s94, s88, 20480\n"
    "s_addc_u32 s95, s89, 0\n"
    "global_load_lds_dwordx4 v2, s[94:95] offset:0 sc1\n"
    "ds_read_b64_tr_b16 v[96:97], v19 offset:16384\n"
    "ds_read_b64_tr_b16 v[98:99], v19 offset:16640\n"
    "v_mul_f32 v139, v139, v27\n"
    "v_cvt_pk_bf16_f32 v37, v138, v139\n"
    "v_fma_f32 v24, v24, v33, v32\n"
    "v_fma_f32 v25, v25, v33, v32\n"
    "v_mul_f32 v24, v132, v24\n"
    "s_waitcnt lgkmcnt(6)\n"
    "v_mfma_f32_32x32x16_bf16 v[112:127], v[196:199], v[100:103], v[112:127]\n"
    "s_getreg_b32 s57, hwreg(HW_REG_IB_STS, 0, 4)\n"
    "s_or_b32 s57, s57, s75\n"
    "s_cmp_gt_u32 s57, 8\n"
    "s_cbranch_scc1 .Lk_s113\n"
    "s_add_u32 s46, s86, 0\n"
    "s_addc_u32 s47, s87, 0\n"
    "s_waitcnt vmcnt(8)\n"
    "s_mov_b64 exec, 1\n"
    "global_store_dwordx2 v3, v[4:5], s[46:47] sc1\n"
    "s_mov_b64 exec, -1\n"
    "s_mov_b32 s75, 64\n"
    ".Lk_s113:\n"
    "ds_read_b64_tr_b16 v[100:101], v19 offset:17408\n"
    "ds_read_b64_tr_b16 v[102:103], v19 offset:17664\n"
    "v_mul_f32 v25, v133, v25\n"
    "v_exp_f32 v24, v24\n"
    "v_exp_f32 v25, v25\n"
    "s_waitcnt lgkmcnt(6)\n"
    "v_mfma_f32_32x32x16_bf16 v[112:127], v[200:203], v[104:107], v[112:127]\n"
    "s_add_u32 m0, s43, 0\n"
    "s_nop 0\n"
    "global_load_lds_dwordx4 v2, s[94:95] offset:1024 sc1\n"
    "ds_read_b64_tr_b16 v[104:105], v19 offset:18432\n"
    "ds_read_b64_tr_b16 v[106:107], v19 offset:18688\n"
    "v_mul_f32 v140, v132, v140\n"
    "v_mul_f32 v141, v133, v141\n"
    "v_fma_f32 v24, v24, v34, v34\n"
    "v_fma_f32 v25, v25, v34, v34\n"
    "v_rcp_f32 v24, v24\n"
    "s_waitcnt lgkmcnt(6)\n"
    "v_mfma_f32_32x32x16_bf16 v[112:127], v[204:207], v[108:111], v[112:127]\n"
    "ds_read_b64_tr_b16 v[108:109], v19 offset:19456\n"
    "ds_read_b64_tr_b16 v[110:111], v19 offset:19712\n"
    "v_rcp_f32 v25, v25\n"
    "v_mul_f32 v26, v134, v134\n"
    "v_mul_f32 v27, v135, v135\n"
    "v_mul_f32 v140, v140, v24\n"
    "s_waitcnt lgkmcnt(6)\n"
    "v_mfma_f32_32x32x16_bf16 v[112:127], v[208:211], v[96:99], v[112:127]\n"
    "s_add_u32 m0, s43, 0\n"
    "s_nop 0\n"
    "global_load_lds_dwordx4 v2, s[94:95] offset:2048 sc1\n"
    "s_cmp_lg_u32 s75, 0\n"
    "s_cbranch_scc1 .Lk_s114\n"
    "s_add_u32 s46, s86, 0\n"
    "s_addc_u32 s47, s87, 0\n"
    "s_waitcnt vmcnt(10)\n"
    "s_mov_b64 exec, 1\n"
    "global_store_dwordx2 v3, v[4:5], s[46:47] sc1\n"
    "s_mov_b64 exec, -1\n"
    "s_mov_b32 s75, 64\n"
    ".Lk_s114:\n"
    "ds_read_b64_tr_b16 v[96:97], v19 offset:20480\n"
    "ds_read_b64_tr_b16 v[98:99], v19 offset:20736\n"
    "v_mul_f32 v141, v141, v25\n"
    "v_cvt_pk_bf16_f32 v38, v140, v141\n"
    "v_fma_f32 v26, v26, v33, v32\n"
    "v_fma_f32 v27, v27, v33, v32\n"
    "v_mul_f32 v26, v134, v26\n"
    "s_waitcnt lgkmcnt(6)\n"
    "v_mfma_f32_32x32x16_bf16 v[112:127], v[212:215], v[100:103], v[112:127]\n"
    "ds_read_b64_tr_b16 v[100:101], v19 offset:21504\n"
    "ds_read_b64_tr_b16 v[102:103], v19 offset:21760\n"
    "v_mul_f32 v27, v135, v27\n"
    "v_exp_f32 v26, v26\n"
    "v_exp_f32 v27, v27\n"
    "s_waitcnt lgkmcnt(6)\n"
    "v_mfma_f32_32x32x16_bf16 v[112:127], v[216:219], v[104:107], v[112:127]\n"
    "s_add_u32 m0, s43, 0\n"
    "s_nop 0\n"
    "global_load_lds_dwordx4 v2, s[94:95] offset:3072 sc1\n"
    "ds_read_b64_tr_b16 v[104:105], v19 offset:22528\n"
    "ds_read_b64_tr_b16 v[106:107], v19 offset:22784\n"
    "v_mul_f32 v142, v134, v142\n"
    "v_mul_f32 v143, v135, v143\n"
    "v_fma_f32 v26, v26, v34, v34\n"
    "v_fma_f32 v27, v27, v34, v34\n"
    "s_waitcnt lgkmcnt(6)\n"
    "v_mfma_f32_32x32x16_bf16 v[112:127], v[220:223], v[108:111], v[112:127]\n"
    "ds_read_b64_tr_b16 v[108:109], v19 offset:23552\n"
    "ds_read_b64_tr_b16 v[110:111], v19 offset:23808\n"
    "v_rcp_f32 v26, v26\n"
    "v_rcp_f32 v27, v27\n"
    "s_waitcnt lgkmcnt(6)\n"
    "v_mfma_f32_32x32x16_bf16 v[112:127], v[224:227], v[96:99], v[112:127]\n"
    "ds_read_b64_tr_b16 v[96:97], v19 offset:24576\n"
    "ds_read_b64_tr_b16 v[98:99], v19 offset:24832\n"
    "v_mul_f32 v142, v142, v26\n"
    "v_mul_f32 v143, v143, v27\n"
    "v_cvt_pk_bf16_f32 v39, v142, v143\n"
    "s_add_u32 s46, s84, 1835008\n"
    "s_addc_u32 s47, s85, 0\n"
    "global_store_dwordx4 v9, v[36:39], s[46:47] sc1\n"
    "s_mov_b32 s75, 0\n"
    "s_waitcnt lgkmcnt(6)\n"
    "v_mfma_f32_32x32x16_bf16 v[112:127], v[228:231], v[100:103], v[112:127]\n"
    "ds_read_b64_tr_b16 v[100:101], v19 offset:25600\n"
    "ds_read_b64_tr_b16 v[102:103], v19 offset:25856\n"
    "s_waitcnt lgkmcnt(6)\n"
    "v_mfma_f32_32x32x16_bf16 v[112:127], v[232:235], v[104:107], v[112:127]\n"
    "ds_read_b64_tr_b16 v[104:105], v19 offset:26624\n"
    "ds_read_b64_tr_b16 v[106:107], v19 offset:26880\n"
    "s_waitcnt lgkmcnt(6)\n"
    "v_mfma_f32_32x32x16_bf16 v[112:127], v[236:239], v[108:111], v[112:127]\n"
    "s_waitcnt vmcnt(10)\n"
    "s_barrier\n"
    "ds_read_b64_tr_b16 v[108:109], v19 offset:27648\n"
    "ds_read_b64_tr_b16 v[110:111], v19 offset:27904\n"
    "s_waitcnt lgkmcnt(6)\n"
    "v_mfma_f32_32x32x16_bf16 v[112:127], v[240:243], v[96:99], v[112:127]\n"
    "ds_read_b64_tr_b16 v[96:97], v6 offset:0\n"
    "ds_read_b64_tr_b16 v[98:99], v6 offset:256\n"
    "s_waitcnt lgkmcnt(6)\n"
    "v_mfma_f32_32x32x16_bf16 v[112:127], v[244:247], v[100:103], v[112:127]\n"
    "ds_read_b64_tr_b16 v[100:101], v6 offset:1024\n"
    "ds_read_b64_tr_b16 v[102:103], v6 offset:1280\n"
    "s_waitcnt lgkmcnt(6)\n"
    "v_mfma_f32_32x32x16_bf16 v[112:127], v[248:251], v[104:107], v[112:127]\n"
    "ds_read_b64_tr_b16 v[104:105], v6 offset:2048\n"
    "ds_read_b64_tr_b16 v[106:107], v6 offset:2304\n"
    "s_waitcnt lgkmcnt(6)\n"
    "v_mfma_f32_32x32x16_bf16 v[112:127], v[252:255], v[108:111], v[112:127]\n"
    "ds_read_b64_tr_b16 v[108:109], v6 offset:3072\n"
    "ds_read_b64_tr_b16 v[110:111], v6 offset:3328\n"
    "s_waitcnt lgkmcnt(6)\n"
    "v_mfma_f32_32x32x16_bf16 v[128:143], v[144:147], v[96:99], 0\n"
    "s_getreg_b32 s57, hwreg(HW_REG_IB_STS, 0, 4)\n"
    "s_or_b32 s57, s57, s75\n"
    "s_cmp_gt_u32 s57, 0\n"
    "s_cbranch_scc1 .Lk_s115\n"
    "s_add_u32 s46, s86, 32768\n"
    "s_addc_u32 s47, s87, 0\n"
    "s_waitcnt vmcnt(0)\n"
    "s_mov_b64 exec, 1\n"
    "global_store_dwordx2 v3, v[4:5], s[46:47] sc1\n"
    "s_mov_b64 exec, -1\n"
    "s_mov_b32 s75, 64\n"
    ".Lk_s115:\n"
    "ds_read_b64_tr_b16 v[96:97], v6 offset:4096\n"
    "ds_read_b64_tr_b16 v[98:99], v6 offset:4352\n"
    "s_waitcnt lgkmcnt(6)\n"
    "v_mfma_f32_32x32x16_bf16 v[128:143], v[148:151], v[100:103], v[128:143]\n"
    "ds_read_b64_tr_b16 v[100:101], v6 offset:5120\n"
    "ds_read_b64_tr_b16 v[102:103], v6 offset:5376\n"
    "s_waitcnt vmcnt(5)\n"
    "v_cmp_ne_u32 vcc, s34, v43\n"
    "s_cbranch_vccnz .Lk_r116\n"
    ".Lk_c117:\n"
    "v_perm_b32 v44, v42, v42, s65\n"
    "v_and_b32 v34, 0xffff0000, v42\n"
    "v_xor_b32 v44, 0x80008000, v44\n"
    "v_rcp_f32 v33, v34\n"
    "v_mul_f32 v34, v34, v34\n"
    "v_mfma_f32_32x32x8_bf16 v[112:127], v[46:47], v[44:45], v[112:127]\n"
    "s_waitcnt lgkmcnt(6)\n"
    "v_mfma_f32_32x32x16_bf16 v[128:143], v[152:155], v[104:107], v[128:143]\n"
    "s_getreg_b32 s57, hwreg(HW_REG_IB_STS, 0, 4)\n"
    "s_or_b32 s57, s57, s75\n"
    "s_cmp_gt_u32 s57, 0\n"
    "s_cbranch_scc1 .Lk_s119\n"
    "s_add_u32 s46, s86, 32768\n"
    "s_addc_u32 s47, s87, 0\n"
    "s_waitcnt vmcnt(0)\n"
    "s_mov_b64 exec, 1\n"
    "global_store_dwordx2 v3, v[4:5], s[46:47] sc1\n"
    "s_mov_b64 exec, -1\n"
    "s_mov_b32 s75, 64\n"
    ".Lk_s119:\n"
    "ds_read_b64_tr_b16 v[104:105], v6 offset:6144\n"
    "ds_read_b64_tr_b16 v[106:107], v6 offset:6400\n"
    "v_mul_f32 v32, 0xc0135761, v33\n"
    "v_mul_f32 v33, v33, v33\n"
    "v_mul_f32 v33, v33, v32\n"
    "v_mul_f32 v33, 0x3d372713, v33\n"
    "s_waitcnt lgkmcnt(6)\n"
    "v_mfma_f32_32x32x16_bf16 v[128:143], v[156:159], v[108:111], v[128:143]\n"
    "s_barrier\n"
    "s_add_u32 m0, s44, 0\n"
    "s_add_u32 s94, s88, 24576\n"
    "s_addc_u32 s95, s89, 0\n"
    "global_load_lds_dwordx4 v2, s[94:95] offset:0 sc1\n"
    "ds_read_b64_tr_b16 v[108:109], v6 offset:7168\n"
    "ds_read_b64_tr_b16 v[110:111], v6 offset:7424\n"
    "s_waitcnt lgkmcnt(6)\n"
    "v_mfma_f32_32x32x16_bf16 v[128:143], v[160:163], v[96:99], v[128:143]\n"
    "ds_read_b128 v[144:147], v10 offset:0\n"
    "ds_read_b64_tr_b16 v[96:97], v6 offset:8192\n"
    "ds_read_b64_tr_b16 v[98:99], v6 offset:8448\n"
    "v_mul_f32 v24, v112, v112\n"
    "v_mul_f32 v25, v113, v113\n"
    "v_fma_f32 v24, v24, v33, v32\n"
    "v_fma_f32 v25, v25, v33, v32\n"
    "v_mul_f32 v24, v112, v24\n"
    "v_mul_f32 v25, v113, v25\n"
    "s_waitcnt lgkmcnt(7)\n"
    "v_mfma_f32_32x32x16_bf16 v[128:143], v[164:167], v[100:103], v[128:143]\n"
    "s_add_u32 m0, s44, 0\n"
    "s_nop 0\n"
    "global_load_lds_dwordx4 v2, s[94:95] offset:1024 sc1\n"
    "ds_read_b128 v[148:151], v10 offset:1024\n"
    "ds_read_b64_tr_b16 v[100:101], v6 offset:9216\n"
    "ds_read_b64_tr_b16 v[102:103], v6 offset:9472\n"
    "v_exp_f32 v24, v24\n"
    "v_exp_f32 v25, v25\n"
    "v_mul_f32 v120, v112, v120\n"
    "v_mul_f32 v121, v113, v121\n"
    "s_waitcnt lgkmcnt(8)\n"
    "v_mfma_f32_32x32x16_bf16 v[128:143], v[168:171], v[104:107], v[128:143]\n"
    "s_add_u32 s52, s22, 13156352\n"
    "s_addc_u32 s53, s23, 0\n"
    "global_load_dwordx2 v[40:41], v30, s[52:53] sc1\n"
    "s_cmp_lg_u32 s75, 0\n"
    "s_cbranch_scc1 .Lk_s120\n"
    "s_add_u32 s46, s86, 32768\n"
    "s_addc_u32 s47, s87, 0\n"
    "s_waitcnt vmcnt(3)\n"
    "s_mov_b64 exec, 1\n"
    "global_store_dwordx2 v3, v[4:5], s[46:47] sc1\n"
    "s_mov_b64 exec, -1\n"
    "s_mov_b32 s75, 64\n"
    ".Lk_s120:\n"
    "ds_read_b128 v[152:155], v10 offset:2048\n"
    "ds_read_b64_tr_b16 v[104:105], v6 offset:10240\n"
    "ds_read_b64_tr_b16 v[106:107], v6 offset:10496\n"
    "v_fma_f32 v24, v24, v34, v34\n"
    "v_fma_f32 v25, v25, v34, v34\n"
    "v_rcp_f32 v24, v24\n"
    "v_rcp_f32 v25, v25\n"
    "s_waitcnt lgkmcnt(9)\n"
    "v_mfma_f32_32x32x16_bf16 v[128:143], v[172:175], v[108:111], v[128:143]\n"
    "s_add_u32 m0, s44, 0\n"
    "s_nop 0\n"
    "global_load_lds_dwordx4 v2, s[94:95] offset:2048 sc1\n"
    "ds_read_b128 v[156:159], v10 offset:3072\n"
    "ds_read_b64_tr_b16 v[108:109], v6 offset:11264\n"
    "ds_read_b64_tr_b16 v[110:111], v6 offset:11520\n"
    "v_mul_f32 v26, v114, v114\n"
    "v_mul_f32 v27, v115, v115\n"
    "v_mul_f32 v120, v120, v24\n"
    "v_mul_f32 v121, v121, v25\n"
    "v_cvt_pk_bf16_f32 v36, v120, v121\n"
    "v_fma_f32 v26, v26, v33, v32\n"
    "s_waitcnt lgkmcnt(9)\n"
    "v_mfma_f32_32x32x16_bf16 v[128:143], v[176:179], v[96:99], v[128:143]\n"
    "ds_read_b128 v[160:163], v10 offset:4096\n"
    "ds_read_b64_tr_b16 v[96:97], v6 offset:12288\n"
    "ds_read_b64_tr_b16 v[98:99], v6 offset:12544\n"
    "v_fma_f32 v27, v27, v33, v32\n"
    "v_mul_f32 v26, v114, v26\n"
    "v_mul_f32 v27, v115, v27\n"
    "v_exp_f32 v26, v26\n"
    "v_exp_f32 v27, v27\n"
    "s_waitcnt lgkmcnt(9)\n"
    "v_mfma_f32_32x32x16_bf16 v[128:143], v[180:183], v[100:103], v[128:143]\n"
    "s_add_u32 m0, s44, 0\n"
    "s_nop 0\n"
    "global_load_lds_dwordx4 v2, s[94:95] offset:3072 sc1\n"
    "ds_read_b128 v[164:167], v10 offset:5120\n"
    "ds_read_b64_tr_b16 v[100:101], v6 offset:13312\n"
    "ds_read_b64_tr_b16 v[102:103], v6 offset:13568\n"
    "v_mul_f32 v122, v114, v122\n"
    "v_mul_f32 v123, v115, v123\n"
    "v_fma_f32 v26, v26, v34, v34\n"
    "v_fma_f32 v27, v27, v34, v34\n"
    "v_rcp_f32 v26, v26\n"
    "s_waitcnt lgkmcnt(9)\n"
    "v_mfma_f32_32x32x16_bf16 v[128:143], v[184:187], v[104:107], v[128:143]\n"
    "s_add_u32 s50, s82, 12288\n"
    "s_addc_u32 s51, s83, 0\n"
    "global_load_dwordx2 v[42:43], v8, s[50:51] sc1\n"
    "ds_read_b128 v[168:171], v10 offset:6144\n"
    "ds_read_b64_tr_b16 v[104:105], v6 offset:14336\n"
    "ds_read_b64_tr_b16 v[106:107], v6 offset:14592\n"
    "v_rcp_f32 v27, v27\n"
    "v_mul_f32 v24, v116, v116\n"
    "v_mul_f32 v25, v117, v117\n"
    "v_mul_f32 v122, v122, v26\n"
    "v_mul_f32 v123, v123, v27\n"
    "s_waitcnt lgkmcnt(9)\n"
    "v_mfma_f32_32x32x16_bf16 v[128:143], v[188:191], v[108:111], v[128:143]\n"
    "ds_read_b128 v[172:175], v10 offset:7168\n"
    "ds_read_b64_tr_b16 v[108:109], v6 offset:15360\n"
    "ds_read_b64_tr_b16 v[110:111], v6 offset:15616\n"
    "v_cvt_pk_bf16_f32 v37, v122, v123\n"
    "v_fma_f32 v24, v24, v33, v32\n"
    "v_fma_f32 v25, v25, v33, v32\n"
    "v_mul_f32 v24, v116, v24\n"
    "v_mul_f32 v25, v117, v25\n"
    "v_exp_f32 v24, v24\n"
    "s_waitcnt lgkmcnt(9)\n"
    "v_mfma_f32_32x32x16_bf16 v[128:143], v[192:195], v[96:99], v[128:143]\n"
    "s_waitcnt vmcnt(7)\n"
    "ds_read_b128 v[176:179], v11 offset:0\n"
    "ds_read_b64_tr_b16 v[96:97], v6 offset:16384\n"
    "ds_read_b64_tr_b16 v[98:99], v6 offset:16640\n"
    "v_exp_f32 v25, v25\n"
    "v_mul_f32 v124, v116, v124\n"
    "v_mul_f32 v125, v117, v125\n"
    "v_fma_f32 v24, v24, v34, v34\n"
    "v_fma_f32 v25, v25, v34, v34\n"
    "s_waitcnt lgkmcnt(9)\n"
    "v_mfma_f32_32x32x16_bf16 v[128:143], v[196:199], v[100:103], v[128:143]\n"
    "ds_read_b128 v[180:183], v11 offset:1024\n"
    "ds_read_b64_tr_b16 v[100:101], v6 offset:17408\n"
    "ds_read_b64_tr_b16 v[102:103], v6 offset:17664\n"
    "v_rcp_f32 v24, v24\n"
    "v_rcp_f32 v25, v25\n"
    "v_mul_f32 v26, v118, v118\n"
    "v_mul_f32 v27, v119, v119\n"
    "s_waitcnt lgkmcnt(9)\n"
    "v_mfma_f32_32x32x16_bf16 v[128:143], v[200:203], v[104:107], v[128:143]\n"
    "ds_read_b128 v[184:187], v11 offset:2048\n"
    "ds_read_b64_tr_b16 v[104:105], v6 offset:18432\n"
    "ds_read_b64_tr_b16 v[106:107], v6 offset:18688\n"
    "v_mul_f32 v124, v124, v24\n"
    "v_mul_f32 v125, v125, v25\n"
    "v_cvt_pk_bf16_f32 v38, v124, v125\n"
    "v_fma_f32 v26, v26, v33, v32\n"
    "v_fma_f32 v27, v27, v33, v32\n"
    "v_mul_f32 v26, v118, v26\n"
    "s_waitcnt lgkmcnt(9)\n"
    "v_mfma_f32_32x32x16_bf16 v[128:143], v[204:207], v[108:111], v[128:143]\n"
    "ds_read_b128 v[188:191], v11 offset:3072\n"
    "ds_read_b64_tr_b16 v[108:109], v6 offset:19456\n"
    "ds_read_b64_tr_b16 v[110:111], v6 offset:19712\n"
    "v_mul_f32 v27, v119, v27\n"
    "v_exp_f32 v26, v26\n"
    "v_exp_f32 v27, v27\n"
    "v_mul_f32 v126, v118, v126\n"
    "s_waitcnt lgkmcnt(9)\n"
    "v_mfma_f32_32x32x16_bf16 v[128:143], v[208:211], v[96:99], v[128:143]\n"
    "ds_read_b64_tr_b16 v[96:97], v6 offset:20480\n"
    "ds_read_b64_tr_b16 v[98:99], v6 offset:20736\n"
    "v_mul_f32 v127, v119, v127\n"
    "v_fma_f32 v26, v26, v34, v34\n"
    "v_fma_f32 v27, v27, v34, v34\n"
    "v_rcp_f32 v26, v26\n"
    "s_waitcnt lgkmcnt(8)\n"
    "v_mfma_f32_32x32x16_bf16 v[128:143], v[212:215], v[100:103], v[128:143]\n"
    "ds_read_b64_tr_b16 v[100:101], v6 offset:21504\n"
    "ds_read_b64_tr_b16 v[102:103], v6 offset:21760\n"
    "v_rcp_f32 v27, v27\n"
    "v_mul_f32 v126, v126, v26\n"
    "v_mul_f32 v127, v127, v27\n"
    "v_cvt_pk_bf16_f32 v39, v126, v127\n"
    "s_add_u32 s46, s84, 3670016\n"
    "s_addc_u32 s47, s85, 0\n"
    "global_store_dwordx4 v9, v[36:39], s[46:47] sc1\n"
    "s_mov_b32 s75, 0\n"
    "s_waitcnt lgkmcnt(7)\n"
    "v_mfma_f32_32x32x16_bf16 v[128:143], v[216:219], v[104:107], v[128:143]\n"
    "ds_read_b64_tr_b16 v[104:105], v6 offset:22528\n"
    "ds_read_b64_tr_b16 v[106:107], v6 offset:22784\n"
    "s_waitcnt lgkmcnt(6)\n"
    "v_mfma_f32_32x32x16_bf16 v[128:143], v[220:223], v[108:111], v[128:143]\n"
    "s_waitcnt vmcnt(4)\n"
    "v_cmp_ne_u64 vcc, s[34:35], v[40:41]\n"
    "s_cbranch_vccnz .Lk_r121\n"
    ".Lk_c122:\n"
    "s_cmp_ge_u32 s28, 4\n"
    "s_cbranch_scc1 .Lk_s124\n"
    "s_add_u32 m0, s42, 0\n"
    "s_add_u32 s92, s68, 0\n"
    "s_addc_u32 s93, s69, 0\n"
    "global_load_lds_dwordx4 v2, s[92:93] offset:0 sc1\n"
    ".Lk_s124:\n"
    "s_add_u32 s94, s70, 0\n"
    "s_addc_u32 s95, s71, 0\n"
    "global_load_dwordx4 v[208:211], v2, s[94:95] offset:0 sc1\n"
    "ds_read_b64_tr_b16 v[108:109], v6 offset:23552\n"
    "ds_read_b64_tr_b16 v[110:111], v6 offset:23808\n"
    "s_waitcnt lgkmcnt(6)\n"
    "v_mfma_f32_32x32x16_bf16 v[128:143], v[224:227], v[96:99], v[128:143]\n"
    "global_load_dwordx4 v[212:215], v2, s[94:95] offset:1024 sc1\n"
    "ds_read_b64_tr_b16 v[96:97], v6 offset:24576\n"
    "ds_read_b64_tr_b16 v[98:99], v6 offset:24832\n"
    "s_waitcnt lgkmcnt(6)\n"
    "v_mfma_f32_32x32x16_bf16 v[128:143], v[228:231], v[100:103], v[128:143]\n"
    "global_load_dwordx4 v[216:219], v2, s[94:95] offset:2048 sc1\n"
    "ds_read_b64_tr_b16 v[100:101], v6 offset:25600\n"
    "ds_read_b64_tr_b16 v[102:103], v6 offset:25856\n"
    "s_waitcnt lgkmcnt(6)\n"
    "v_mfma_f32_32x32x16_bf16 v[128:143], v[232:235], v[104:107], v[128:143]\n"
    "global_load_dwordx4 v[220:223], v2, s[94:95] offset:3072 sc1\n"
    "ds_read_b64_tr_b16 v[104:105], v6 offset:26624\n"
    "ds_read_b64_tr_b16 v[106:107], v6 offset:26880\n"
    "s_waitcnt lgkmcnt(6)\n"
    "v_mfma_f32_32x32x16_bf16 v[128:143], v[236:239], v[108:111], v[128:143]\n"
    "ds_read_b64_tr_b16 v[108:109], v6 offset:27648\n"
    "ds_read_b64_tr_b16 v[110:111], v6 offset:27904\n"
    "s_waitcnt lgkmcnt(6)\n"
    "v_mfma_f32_32x32x16_bf16 v[128:143], v[240:243], v[96:99], v[128:143]\n"
    "s_add_u32 s94, s70, 4096\n"
    "s_addc_u32 s95, s71, 0\n"
    "global_load_dwordx4 v[224:227], v2, s[94:95] offset:0 sc1\n"
    "s_waitcnt lgkmcnt(4)\n"
    "v_mfma_f32_32x32x16_bf16 v[128:143], v[244:247], v[100:103], v[128:143]\n"
    "global_load_dwordx4 v[228:231], v2, s[94:95] offset:1024 sc1\n"
    "s_waitcnt vmcnt(8)\n"
    "ds_read_b128 v[192:195], v12 offset:0\n"
    "s_waitcnt lgkmcnt(3)\n"
    "v_mfma_f32_32x32x16_bf16 v[128:143], v[248:251], v[104:107], v[128:143]\n"
    "ds_read_b128 v[196:199], v12 offset:1024\n"
    "ds_read_b128 v[200:203], v12 offset:2048\n"
    "s_waitcnt lgkmcnt(3)\n"
    "v_mfma_f32_32x32x16_bf16 v[128:143], v[252:255], v[108:111], v[128:143]\n"
    "global_load_dwordx4 v[232:235], v2, s[94:95] offset:2048 sc1\n"
    "ds_read_b128 v[204:207], v12 offset:3072\n"
    "s_waitcnt vmcnt(8)\n"
    "v_cmp_ne_u32 vcc, s34, v43\n"
    "s_cbranch_vccnz .Lk_r125\n"
    ".Lk_c126:\n"
    "v_perm_b32 v44, v42, v42, s65\n"
    "v_and_b32 v34, 0xffff0000, v42\n"
    "v_xor_b32 v44, 0x80008000, v44\n"
    "v_rcp_f32 v33, v34\n"
    "v_mul_f32 v34, v34, v34\n"
    "v_mfma_f32_32x32x8_bf16 v[128:143], v[46:47], v[44:45], v[128:143]\n"
    "v_and_b32 v6, 15, v1\n"
    "v_lshrrev_b32 v7, 4, v1\n"
    "s_lshl_b32 s55, s91, 2\n"
    "v_or_b32 v8, s55, v7\n"
    "v_xor_b32 v8, v8, v6\n"
    "v_lshlrev_b32 v8, 4, v8\n"
    "v_lshl_or_b32 v8, v6, 8, v8\n"
    "s_lshl_b32 s55, s28, 12\n"
    "s_add_u32 s52, s22, 13189120\n"
    "s_addc_u32 s53, s23, 0\n"
    "global_load_dwordx2 v[40:41], v30, s[52:53] sc1\n"
    "s_add_u32 s46, s86, 65536\n"
    "s_addc_u32 s47, s87, 0\n"
    "s_waitcnt vmcnt(8)\n"
    "s_mov_b64 exec, 1\n"
    "global_store_dwordx2 v3, v[4:5], s[46:47] sc1\n"
    "s_mov_b64 exec, -1\n"
    "v_mul_f32 v32, 0xc0135761, v33\n"
    "v_mul_f32 v33, v33, v33\n"
    "v_mul_f32 v33, v33, v32\n"
    "v_mul_f32 v33, 0x3d372713, v33\n"
    "v_mul_f32 v24, v128, v128\n"
    "v_mul_f32 v25, v129, v129\n"
    "v_fma_f32 v24, v24, v33, v32\n"
    "v_fma_f32 v25, v25, v33, v32\n"
    "v_mul_f32 v24, v128, v24\n"
    "v_mul_f32 v25, v129, v25\n"
    "v_exp_f32 v24, v24\n"
    "v_exp_f32 v25, v25\n"
    "v_mul_f32 v136, v128, v136\n"
    "v_mul_f32 v137, v129, v137\n"
    "v_fma_f32 v24, v24, v34, v34\n"
    "v_fma_f32 v25, v25, v34, v34\n"
    "v_rcp_f32 v24, v24\n"
    "v_rcp_f32 v25, v25\n"
    "v_mul_f32 v26, v130, v130\n"
    "v_mul_f32 v27, v131, v131\n"
    "v_mul_f32 v136, v136, v24\n"
    "v_mul_f32 v137, v137, v25\n"
    "v_cvt_pk_bf16_f32 v36, v136, v137\n"
    "v_fma_f32 v26, v26, v33, v32\n"
    "v_fma_f32 v27, v27, v33, v32\n"
    "v_mul_f32 v26, v130, v26\n"
    "v_mul_f32 v27, v131, v27\n"
    "v_exp_f32 v26, v26\n"
    "v_exp_f32 v27, v27\n"
    "v_mul_f32 v138, v130, v138\n"
    "v_mul_f32 v139, v131, v139\n"
    "v_fma_f32 v26, v26, v34, v34\n"
    "v_fma_f32 v27, v27, v34, v34\n"
    "v_rcp_f32 v26, v26\n"
    "v_rcp_f32 v27, v27\n"
    "v_mul_f32 v24, v132, v132\n"
    "v_mul_f32 v25, v133, v133\n"
    "v_mul_f32 v138, v138, v26\n"
    "v_mul_f32 v139, v139, v27\n"
    "v_cvt_pk_bf16_f32 v37, v138, v139\n"
    "v_fma_f32 v24, v24, v33, v32\n"
    "v_fma_f32 v25, v25, v33, v32\n"
    "v_mul_f32 v24, v132, v24\n"
    "v_mul_f32 v25, v133, v25\n"
    "v_exp_f32 v24, v24\n"
    "v_exp_f32 v25, v25\n"
    "v_mul_f32 v140, v132, v140\n"
    "v_mul_f32 v141, v133, v141\n"
    "v_fma_f32 v24, v24, v34, v34\n"
    "v_fma_f32 v25, v25, v34, v34\n"
    "v_rcp_f32 v24, v24\n"
    "v_rcp_f32 v25, v25\n"
    "v_mul_f32 v26, v134, v134\n"
    "v_mul_f32 v27, v135, v135\n"
    "v_mul_f32 v140, v140, v24\n"
    "v_mul_f32 v141, v141, v25\n"
    "v_cvt_pk_bf16_f32 v38, v140, v141\n"
    "v_fma_f32 v26, v26, v33, v32\n"
    "v_fma_f32 v27, v27, v33, v32\n"
    "v_mul_f32 v26, v134, v26\n"
    "v_mul_f32 v27, v135, v27\n"
    "v_exp_f32 v26, v26\n"
    "v_exp_f32 v27, v27\n"
    "v_mul_f32 v142, v134, v142\n"
    "v_mul_f32 v143, v135, v143\n"
    "v_fma_f32 v26, v26, v34, v34\n"
    "v_fma_f32 v27, v27, v34, v34\n"
    "v_rcp_f32 v26, v26\n"
    "v_rcp_f32 v27, v27\n"
    "v_mul_f32 v142, v142, v26\n"
    "v_mul_f32 v143, v143, v27\n"
    "v_cvt_pk_bf16_f32 v39, v142, v143\n"
    "s_add_u32 s46, s84, 5505024\n"
    "s_addc_u32 s47, s85, 0\n"
    "global_store_dwordx4 v9, v[36:39], s[46:47] sc1\n"
    "s_mov_b32 s75, 0\n"
    "v_xor_b32 v10, 0, v8\n"
    "v_xor_b32 v11, 64, v8\n"
    "v_xor_b32 v12, 128, v8\n"
    "v_xor_b32 v13, 192, v8\n"
    "v_add_u32 v10, s55, v10\n"
    "v_add_u32 v11, s55, v11\n"
    "v_add_u32 v12, s55, v12\n"
    "v_add_u32 v13, s55, v13\n"
    "v_bfe_u32 v8, v0, 4, 4\n"
    "v_xor_b32 v14, v8, v6\n"
    "v_lshlrev_b32 v14, 4, v14\n"
    "v_lshl_or_b32 v14, v8, 8, v14\n"
    "s_lshr_b32 s55, s28, 2\n"
    "s_mul_i32 s55, s55, 98304\n"
    "v_add_u32 v14, s55, v14\n"
    "v_mul_u32_u24 v8, 896, v8\n"
    "v_lshl_add_u32 v15, v6, 3, v8\n"
    "v_lshlrev_b32 v8, 2, v7\n"
    "v_sub_u32 v8, v6, v8\n"
    "v_mov_b32 v22, 0x3f80\n"
    "v_mov_b32 v9, 0x3f800000\n"
    "v_cmp_eq_u32 vcc, 0, v8\n"
    "v_cmp_eq_u32 s[60:61], 1, v8\n"
    "s_nop 0\n"
    "v_cndmask_b32 v16, v3, v22, vcc\n"
    "v_cndmask_b32 v16, v16, v9, s[60:61]\n"
    "v_cmp_eq_u32 vcc, 2, v8\n"
    "v_cmp_eq_u32 s[60:61], 3, v8\n"
    "s_nop 0\n"
    "v_cndmask_b32 v17, v3, v22, vcc\n"
    "v_cndmask_b32 v17, v17, v9, s[60:61]\n"
    "s_mov_b32 s59, 98304\n"
    "s_mov_b32 s75, 0\n"
    "s_waitcnt vmcnt(2)\n"
    "v_cmp_ne_u64 vcc, s[34:35], v[40:41]\n"
    "s_cbranch_vccnz .Lk_us128\n"
    "v_mfma_f32_16x16x32_bf16 v[96:99], v[48:51], v[208:211], 0\n"
    "v_mfma_f32_16x16x32_bf16 v[100:103], v[52:55], v[208:211], 0\n"
    "v_mfma_f32_16x16x32_bf16 v[104:107], v[56:59], v[208:211], 0\n"
    "v_mfma_f32_16x16x32_bf16 v[108:111], v[60:63], v[208:211], 0\n"
    "s_cmp_ge_u32 s28, 4\n"
    "s_cbranch_scc1 .Lk_s130\n"
    "s_add_u32 m0, s42, 1024\n"
    "s_add_u32 s92, s68, 458752\n"
    "s_addc_u32 s93, s69, 0\n"
    "global_load_lds_dwordx4 v2, s[92:93] offset:0 sc1\n"
    ".Lk_s130:\n"
    "s_add_u32 s94, s70, 1835008\n"
    "s_addc_u32 s95, s71, 0\n"
    "global_load_dwordx4 v[112:115], v2, s[94:95] offset:0 sc1\n"
    "v_mfma_f32_16x16x32_bf16 v[96:99], v[64:67], v[212:215], v[96:99]\n"
    "v_mfma_f32_16x16x32_bf16 v[100:103], v[68:71], v[212:215], v[100:103]\n"
    "v_mfma_f32_16x16x32_bf16 v[104:107], v[72:75], v[212:215], v[104:107]\n"
    "v_mfma_f32_16x16x32_bf16 v[108:111], v[76:79], v[212:215], v[108:111]\n"
    "global_load_dwordx4 v[116:119], v2, s[94:95] offset:1024 sc1\n"
    "s_cmp_ge_u32 s28, 4\n"
    "s_cbranch_scc1 .Lk_s131\n"
    "ds_read_b64_tr_b16 v[6:7], v31 offset:0\n"
    "s_waitcnt lgkmcnt(0)\n"
    "v_mfma_f32_16x16x16_bf16 v[96:99], v[16:17], v[6:7], v[96:99]\n"
    "s_nop 5\n"
    ".Lk_s131:\n"
    "s_getreg_b32 s57, hwreg(HW_REG_IB_STS, 0, 4)\n"
    "s_or_b32 s57, s57, s75\n"
    "s_cmp_gt_u32 s57, 2\n"
    "s_cbranch_scc1 .Lk_s132\n"
    "s_add_u32 s46, s86, 98304\n"
    "s_addc_u32 s47, s87, 0\n"
    "s_waitcnt vmcnt(2)\n"
    "s_mov_b64 exec, 1\n"
    "global_store_dwordx2 v3, v[4:5], s[46:47] sc1\n"
    "s_mov_b64 exec, -1\n"
    "s_mov_b32 s75, 64\n"
    ".Lk_s132:\n"
    "v_mfma_f32_16x16x32_bf16 v[96:99], v[80:83], v[216:219], v[96:99]\n"
    "v_mfma_f32_16x16x32_bf16 v[100:103], v[84:87], v[216:219], v[100:103]\n"
    "v_mfma_f32_16x16x32_bf16 v[104:107], v[88:91], v[216:219], v[104:107]\n"
    "v_mfma_f32_16x16x32_bf16 v[108:111], v[92:95], v[216:219], v[108:111]\n"
    "global_load_dwordx4 v[120:123], v2, s[94:95] offset:2048 sc1\n"
    "s_getreg_b32 s57, hwreg(HW_REG_IB_STS, 0, 4)\n"
    "s_or_b32 s57, s57, s75\n"
    "s_cmp_gt_u32 s57, 3\n"
    "s_cbranch_scc1 .Lk_s133\n"
    "s_add_u32 s46, s86, 98304\n"
    "s_addc_u32 s47, s87, 0\n"
    "s_waitcnt vmcnt(3)\n"
    "s_mov_b64 exec, 1\n"
    "global_store_dwordx2 v3, v[4:5], s[46:47] sc1\n"
    "s_mov_b64 exec, -1\n"
    "s_mov_b32 s75, 64\n"
    ".Lk_s133:\n"
    "s_add_u32 s52, s22, 13221888\n"
    "s_addc_u32 s53, s23, 0\n"
    "global_load_dwordx2 v[42:43], v30, s[52:53] sc1\n"
    "v_mfma_f32_16x16x32_bf16 v[96:99], v[144:147], v[220:223], v[96:99]\n"
    "v_mfma_f32_16x16x32_bf16 v[100:103], v[148:151], v[220:223], v[100:103]\n"
    "v_mfma_f32_16x16x32_bf16 v[104:107], v[152:155], v[220:223], v[104:107]\n"
    "v_mfma_f32_16x16x32_bf16 v[108:111], v[156:159], v[220:223], v[108:111]\n"
    "global_load_dwordx4 v[124:127], v2, s[94:95] offset:3072 sc1\n"
    "v_mfma_f32_16x16x32_bf16 v[96:99], v[160:163], v[224:227], v[96:99]\n"
    "v_mfma_f32_16x16x32_bf16 v[100:103], v[164:167], v[224:227], v[100:103]\n"
    "v_mfma_f32_16x16x32_bf16 v[104:107], v[168:171], v[224:227], v[104:107]\n"
    "v_mfma_f32_16x16x32_bf16 v[108:111], v[172:175], v[224:227], v[108:111]\n"
    "s_add_u32 s94, s70, 1839104\n"
    "s_addc_u32 s95, s71, 0\n"
    "global_load_dwordx4 v[236:239], v2, s[94:95] offset:0 sc1\n"
    "s_getreg_b32 s57, hwreg(HW_REG_IB_STS, 0, 4)\n"
    "s_or_b32 s57, s57, s75\n"
    "s_cmp_gt_u32 s57, 6\n"
    "s_cbranch_scc1 .Lk_s134\n"
    "s_add_u32 s46, s86, 98304\n"
    "s_addc_u32 s47, s87, 0\n"
    "s_waitcnt vmcnt(6)\n"
    "s_mov_b64 exec, 1\n"
    "global_store_dwordx2 v3, v[4:5], s[46:47] sc1\n"
    "s_mov_b64 exec, -1\n"
    "s_mov_b32 s75, 64\n"
    ".Lk_s134:\n"
    "v_mfma_f32_16x16x32_bf16 v[96:99], v[176:179], v[228:231], v[96:99]\n"
    "v_mfma_f32_16x16x32_bf16 v[100:103], v[180:183], v[228:231], v[100:103]\n"
    "v_mfma_f32_16x16x32_bf16 v[104:107], v[184:187], v[228:231], v[104:107]\n"
    "v_mfma_f32_16x16x32_bf16 v[108:111], v[188:191], v[228:231], v[108:111]\n"
    "global_load_dwordx4 v[240:243], v2, s[94:95] offset:1024 sc1\n"
    "s_getreg_b32 s57, hwreg(HW_REG_IB_STS, 0, 4)\n"
    "s_or_b32 s57, s57, s75\n"
    "s_cmp_gt_u32 s57, 7\n"
    "s_cbranch_scc1 .Lk_s135\n"
    "s_add_u32 s46, s86, 98304\n"
    "s_addc_u32 s47, s87, 0\n"
    "s_waitcnt vmcnt(7)\n"
    "s_mov_b64 exec, 1\n"
    "global_store_dwordx2 v3, v[4:5], s[46:47] sc1\n"
    "s_mov_b64 exec, -1\n"
    "s_mov_b32 s75, 64\n"
    ".Lk_s135:\n"
    "s_waitcnt lgkmcnt(3)\n"
    "v_mfma_f32_16x16x32_bf16 v[96:99], v[192:195], v[232:235], v[96:99]\n"
    "s_waitcnt lgkmcnt(2)\n"
    "v_mfma_f32_16x16x32_bf16 v[100:103], v[196:199], v[232:235], v[100:103]\n"
    "s_waitcnt lgkmcnt(1)\n"
    "v_mfma_f32_16x16x32_bf16 v[104:107], v[200:203], v[232:235], v[104:107]\n"
    "s_waitcnt lgkmcnt(0)\n"
    "v_mfma_f32_16x16x32_bf16 v[108:111], v[204:207], v[232:235], v[108:111]\n"
    "global_load_dwordx4 v[244:247], v2, s[94:95] offset:2048 sc1\n"
    "s_getreg_b32 s57, hwreg(HW_REG_IB_STS, 0, 4)\n"
    "s_or_b32 s57, s57, s75\n"
    "s_cmp_gt_u32 s57, 8\n"
    "s_cbranch_scc1 .Lk_s136\n"
    "s_add_u32 s46, s86, 98304\n"
    "s_addc_u32 s47, s87, 0\n"
    "s_waitcnt vmcnt(8)\n"
    "s_mov_b64 exec, 1\n"
    "global_store_dwordx2 v3, v[4:5], s[46:47] sc1\n"
    "s_mov_b64 exec, -1\n"
    "s_mov_b32 s75, 64\n"
    ".Lk_s136:\n"
    "s_branch .Lk_um129\n"
    ".Lk_us128:\n"
    "v_mfma_f32_16x16x32_bf16 v[96:99], v[48:51], v[208:211], 0\n"
    "v_mfma_f32_16x16x32_bf16 v[100:103], v[52:55], v[208:211], 0\n"
    "v_mfma_f32_16x16x32_bf16 v[104:107], v[56:59], v[208:211], 0\n"
    "v_mfma_f32_16x16x32_bf16 v[108:111], v[60:63], v[208:211], 0\n"
    "v_mfma_f32_16x16x32_bf16 v[96:99], v[64:67], v[212:215], v[96:99]\n"
    "v_mfma_f32_16x16x32_bf16 v[100:103], v[68:71], v[212:215], v[100:103]\n"
    "v_mfma_f32_16x16x32_bf16 v[104:107], v[72:75], v[212:215], v[104:107]\n"
    "v_mfma_f32_16x16x32_bf16 v[108:111], v[76:79], v[212:215], v[108:111]\n"
    "s_cmp_ge_u32 s28, 4\n"
    "s_cbranch_scc1 .Lk_s137\n"
    "ds_read_b64_tr_b16 v[6:7], v31 offset:0\n"
    "s_waitcnt lgkmcnt(0)\n"
    "v_mfma_f32_16x16x16_bf16 v[96:99], v[16:17], v[6:7], v[96:99]\n"
    "s_nop 5\n"
    ".Lk_s137:\n"
    "s_getreg_b32 s57, hwreg(HW_REG_IB_STS, 0, 4)\n"
    "s_or_b32 s57, s57, s75\n"
    "s_cmp_gt_u32 s57, 0\n"
    "s_cbranch_scc1 .Lk_s138\n"
    "s_add_u32 s46, s86, 98304\n"
    "s_addc_u32 s47, s87, 0\n"
    "s_waitcnt vmcnt(0)\n"
    "s_mov_b64 exec, 1\n"
    "global_store_dwordx2 v3, v[4:5], s[46:47] sc1\n"
    "s_mov_b64 exec, -1\n"
    "s_mov_b32 s75, 64\n"
    ".Lk_s138:\n"
    "v_mfma_f32_16x16x32_bf16 v[96:99], v[80:83], v[216:219], v[96:99]\n"
    "v_mfma_f32_16x16x32_bf16 v[100:103], v[84:87], v[216:219], v[100:103]\n"
    "v_mfma_f32_16x16x32_bf16 v[104:107], v[88:91], v[216:219], v[104:107]\n"
    "v_mfma_f32_16x16x32_bf16 v[108:111], v[92:95], v[216:219], v[108:111]\n"
    "s_getreg_b32 s57, hwreg(HW_REG_IB_STS, 0, 4)\n"
    "s_or_b32 s57, s57, s75\n"
    "s_cmp_gt_u32 s57, 0\n"
    "s_cbranch_scc1 .Lk_s139\n"
    "s_add_u32 s46, s86, 98304\n"
    "s_addc_u32 s47, s87, 0\n"
    "s_waitcnt vmcnt(0)\n"
    "s_mov_b64 exec, 1\n"
    "global_store_dwordx2 v3, v[4:5], s[46:47] sc1\n"
    "s_mov_b64 exec, -1\n"
    "s_mov_b32 s75, 64\n"
    ".Lk_s139:\n"
    "s_add_u32 s52, s22, 13189120\n"
    "s_addc_u32 s53, s23, 0\n"
    "global_load_dwordx2 v[40:41], v30, s[52:53] sc1\n"
    "v_mfma_f32_16x16x32_bf16 v[96:99], v[144:147], v[220:223], v[96:99]\n"
    "v_mfma_f32_16x16x32_bf16 v[100:103], v[148:151], v[220:223], v[100:103]\n"
    "v_mfma_f32_16x16x32_bf16 v[104:107], v[152:155], v[220:223], v[104:107]\n"
    "v_mfma_f32_16x16x32_bf16 v[108:111], v[156:159], v[220:223], v[108:111]\n"
    "v_mfma_f32_16x16x32_bf16 v[96:99], v[160:163], v[224:227], v[96:99]\n"
    "v_mfma_f32_16x16x32_bf16 v[100:103], v[164:167], v[224:227], v[100:103]\n"
    "v_mfma_f32_16x16x32_bf16 v[104:107], v[168:171], v[224:227], v[104:107]\n"
    "v_mfma_f32_16x16x32_bf16 v[108:111], v[172:175], v[224:227], v[108:111]\n"
    "s_getreg_b32 s57, hwreg(HW_REG_IB_STS, 0, 4)\n"
    "s_or_b32 s57, s57, s75\n"
    "s_cmp_gt_u32 s57, 1\n"
    "s_cbranch_scc1 .Lk_s140\n"
    "s_add_u32 s46, s86, 98304\n"
    "s_addc_u32 s47, s87, 0\n"
    "s_waitcnt vmcnt(1)\n"
    "s_mov_b64 exec, 1\n"
    "global_store_dwordx2 v3, v[4:5], s[46:47] sc1\n"
    "s_mov_b64 exec, -1\n"
    "s_mov_b32 s75, 64\n"
    ".Lk_s140:\n"
    "v_mfma_f32_16x16x32_bf16 v[96:99], v[176:179], v[228:231], v[96:99]\n"
    "v_mfma_f32_16x16x32_bf16 v[100:103], v[180:183], v[228:231], v[100:103]\n"
    "v_mfma_f32_16x16x32_bf16 v[104:107], v[184:187], v[228:231], v[104:107]\n"
    "v_mfma_f32_16x16x32_bf16 v[108:111], v[188:191], v[228:231], v[108:111]\n"
    "s_getreg_b32 s57, hwreg(HW_REG_IB_STS, 0, 4)\n"
    "s_or_b32 s57, s57, s75\n"
    "s_cmp_gt_u32 s57, 1\n"
    "s_cbranch_scc1 .Lk_s141\n"
    "s_add_u32 s46, s86, 98304\n"
    "s_addc_u32 s47, s87, 0\n"
    "s_waitcnt vmcnt(1)\n"
    "s_mov_b64 exec, 1\n"
    "global_store_dwordx2 v3, v[4:5], s[46:47] sc1\n"
    "s_mov_b64 exec, -1\n"
    "s_mov_b32 s75, 64\n"
    ".Lk_s141:\n"
    "s_waitcnt lgkmcnt(3)\n"
    "v_mfma_f32_16x16x32_bf16 v[96:99], v[192:195], v[232:235], v[96:99]\n"
    "s_waitcnt lgkmcnt(2)\n"
    "v_mfma_f32_16x16x32_bf16 v[100:103], v[196:199], v[232:235], v[100:103]\n"
    "s_waitcnt lgkmcnt(1)\n"
    "v_mfma_f32_16x16x32_bf16 v[104:107], v[200:203], v[232:235], v[104:107]\n"
    "s_waitcnt lgkmcnt(0)\n"
    "v_mfma_f32_16x16x32_bf16 v[108:111], v[204:207], v[232:235], v[108:111]\n"
    "s_getreg_b32 s57, hwreg(HW_REG_IB_STS, 0, 4)\n"
    "s_or_b32 s57, s57, s75\n"
    "s_cmp_gt_u32 s57, 1\n"
    "s_cbranch_scc1 .Lk_s142\n"
    "s_add_u32 s46, s86, 98304\n"
    "s_addc_u32 s47, s87, 0\n"
    "s_waitcnt vmcnt(1)\n"
    "s_mov_b64 exec, 1\n"
    "global_store_dwordx2 v3, v[4:5], s[46:47] sc1\n"
    "s_mov_b64 exec, -1\n"
    "s_mov_b32 s75, 64\n"
    ".Lk_s142:\n"
    "s_waitcnt vmcnt(0)\n"
    "v_cmp_ne_u64 vcc, s[34:35], v[40:41]\n"
    "s_cbranch_vccnz .Lk_r143\n"
    ".Lk_c144:\n"
    "s_cmp_ge_u32 s28, 4\n"
    "s_cbranch_scc1 .Lk_s146\n"
    "s_add_u32 m0, s42, 1024\n"
    "s_add_u32 s92, s68, 458752\n"
    "s_addc_u32 s93, s69, 0\n"
    "global_load_lds_dwordx4 v2, s[92:93] offset:0 sc1\n"
    ".Lk_s146:\n"
    "s_add_u32 s94, s70, 1835008\n"
    "s_addc_u32 s95, s71, 0\n"
    "global_load_dwordx4 v[112:115], v2, s[94:95] offset:0 sc1\n"
    "global_load_dwordx4 v[116:119], v2, s[94:95] offset:1024 sc1\n"
    "global_load_dwordx4 v[120:123], v2, s[94:95] offset:2048 sc1\n"
    "global_load_dwordx4 v[124:127], v2, s[94:95] offset:3072 sc1\n"
    "s_add_u32 s94, s70, 1839104\n"
    "s_addc_u32 s95, s71, 0\n"
    "global_load_dwordx4 v[236:239], v2, s[94:95] offset:0 sc1\n"
    "global_load_dwordx4 v[240:243], v2, s[94:95] offset:1024 sc1\n"
    "global_load_dwordx4 v[244:247], v2, s[94:95] offset:2048 sc1\n"
    "s_add_u32 s52, s22, 13221888\n"
    "s_addc_u32 s53, s23, 0\n"
    "global_load_dwordx2 v[42:43], v30, s[52:53] sc1\n"
    ".Lk_um129:\n"
    "s_barrier\n"
    "ds_write_b128 v10, v[96:99]\n"
    "ds_write_b128 v11, v[100:103]\n"
    "ds_write_b128 v12, v[104:107]\n"
    "ds_write_b128 v13, v[108:111]\n"
    "s_cmp_lg_u32 s75, 0\n"
    "s_cbranch_scc1 .Lk_s147\n"
    "s_add_u32 s46, s86, 98304\n"
    "s_addc_u32 s47, s87, 0\n"
    "s_mov_b64 exec, 1\n"
    "global_store_dwordx2 v3, v[4:5], s[46:47] sc1\n"
    "s_mov_b64 exec, -1\n"
    "s_mov_b32 s75, 64\n"
    ".Lk_s147:\n"
    "s_waitcnt lgkmcnt(0)\n"
    "s_barrier\n"
    "s_waitcnt vmcnt(0)\n"
    "v_cmp_ne_u64 vcc, s[34:35], v[42:43]\n"
    "s_cbranch_vccnz .Lk_us148\n"
    "v_mfma_f32_16x16x32_bf16 v[96:99], v[48:51], v[112:115], 0\n"
    "v_mfma_f32_16x16x32_bf16 v[100:103], v[52:55], v[112:115], 0\n"
    "v_mfma_f32_16x16x32_bf16 v[104:107], v[56:59], v[112:115], 0\n"
    "v_mfma_f32_16x16x32_bf16 v[108:111], v[60:63], v[112:115], 0\n"
    "s_cmp_ge_u32 s28, 4\n"
    "s_cbranch_scc1 .Lk_s150\n"
    "s_add_u32 m0, s42, 2048\n"
    "s_add_u32 s92, s68, 917504\n"
    "s_addc_u32 s93, s69, 0\n"
    "global_load_lds_dwordx4 v2, s[92:93] offset:0 sc1\n"
    ".Lk_s150:\n"
    "s_add_u32 s94, s70, 3670016\n"
    "s_addc_u32 s95, s71, 0\n"
    "global_load_dwordx4 v[128:131], v2, s[94:95] offset:0 sc1\n"
    "s_cmp_ge_u32 s28, 4\n"
    "s_cbranch_scc1 .Lk_s151\n"
    "ds_read_b128 v[24:27], v14 offset:0\n"
    "ds_read_b128 v[32:35], v14 offset:4096\n"
    "ds_read_b128 v[36:39], v14 offset:8192\n"
    "ds_read_b128 v[18:21], v14 offset:12288\n"
    ".Lk_s151:\n"
    "v_mfma_f32_16x16x32_bf16 v[96:99], v[64:67], v[116:119], v[96:99]\n"
    "v_mfma_f32_16x16x32_bf16 v[100:103], v[68:71], v[116:119], v[100:103]\n"
    "v_mfma_f32_16x16x32_bf16 v[104:107], v[72:75], v[116:119], v[104:107]\n"
    "v_mfma_f32_16x16x32_bf16 v[108:111], v[76:79], v[116:119], v[108:111]\n"
    "global_load_dwordx4 v[132:135], v2, s[94:95] offset:1024 sc1\n"
    "s_cmp_ge_u32 s28, 4\n"
    "s_cbranch_scc1 .Lk_s152\n"
    "ds_read_b64_tr_b16 v[6:7], v31 offset:1024\n"
    "s_waitcnt lgkmcnt(0)\n"
    "v_mfma_f32_16x16x16_bf16 v[96:99], v[16:17], v[6:7], v[96:99]\n"
    "s_nop 5\n"
    ".Lk_s152:\n"
    "v_mfma_f32_16x16x32_bf16 v[96:99], v[80:83], v[120:123], v[96:99]\n"
    "v_mfma_f32_16x16x32_bf16 v[100:103], v[84:87], v[120:123], v[100:103]\n"
    "v_mfma_f32_16x16x32_bf16 v[104:107], v[88:91], v[120:123], v[104:107]\n"
    "v_mfma_f32_16x16x32_bf16 v[108:111], v[92:95], v[120:123], v[108:111]\n"
    "global_load_dwordx4 v[136:139], v2, s[94:95] offset:2048 sc1\n"
    "s_cmp_ge_u32 s28, 4\n"
    "s_cbranch_scc1 .Lk_s153\n"
    "s_waitcnt lgkmcnt(0)\n"
    "v_add_f32 v24, v24, v32\n"
    "v_add_f32 v25, v25, v33\n"
    "v_add_f32 v26, v26, v34\n"
    "v_add_f32 v27, v27, v35\n"
    "v_add_f32 v24, v24, v36\n"
    "v_add_f32 v25, v25, v37\n"
    "v_add_f32 v26, v26, v38\n"
    "v_add_f32 v27, v27, v39\n"
    "v_add_f32 v24, v24, v18\n"
    "v_add_f32 v25, v25, v19\n"
    "v_add_f32 v26, v26, v20\n"
    "v_add_f32 v27, v27, v21\n"
    "ds_read_b128 v[32:35], v14 offset:16384\n"
    "ds_read_b128 v[36:39], v14 offset:20480\n"
    "ds_read_b128 v[18:21], v14 offset:24576\n"
    ".Lk_s153:\n"
    "s_add_u32 s52, s22, 13254656\n"
    "s_addc_u32 s53, s23, 0\n"
    "global_load_dwordx2 v[40:41], v30, s[52:53] sc1\n"
    "v_mfma_f32_16x16x32_bf16 v[96:99], v[144:147], v[124:127], v[96:99]\n"
    "v_mfma_f32_16x16x32_bf16 v[100:103], v[148:151], v[124:127], v[100:103]\n"
    "v_mfma_f32_16x16x32_bf16 v[104:107], v[152:155], v[124:127], v[104:107]\n"
    "v_mfma_f32_16x16x32_bf16 v[108:111], v[156:159], v[124:127], v[108:111]\n"
    "global_load_dwordx4 v[140:143], v2, s[94:95] offset:3072 sc1\n"
    "v_mfma_f32_16x16x32_bf16 v[96:99], v[160:163], v[236:239], v[96:99]\n"
    "v_mfma_f32_16x16x32_bf16 v[100:103], v[164:167], v[236:239], v[100:103]\n"
    "v_mfma_f32_16x16x32_bf16 v[104:107], v[168:171], v[236:239], v[104:107]\n"
    "v_mfma_f32_16x16x32_bf16 v[108:111], v[172:175], v[236:239], v[108:111]\n"
    "s_add_u32 s94, s70, 3674112\n"
    "s_addc_u32 s95, s71, 0\n"
    "global_load_dwordx4 v[248:251], v2, s[94:95] offset:0 sc1\n"
    "s_cmp_ge_u32 s28, 4\n"
    "s_cbranch_scc1 .Lk_s154\n"
    "s_waitcnt lgkmcnt(0)\n"
    "v_add_f32 v24, v24, v32\n"
    "v_add_f32 v25, v25, v33\n"
    "v_add_f32 v26, v26, v34\n"
    "v_add_f32 v27, v27, v35\n"
    "v_add_f32 v24, v24, v36\n"
    "v_add_f32 v25, v25, v37\n"
    "v_add_f32 v26, v26, v38\n"
    "v_add_f32 v27, v27, v39\n"
    "v_add_f32 v24, v24, v18\n"
    "v_add_f32 v25, v25, v19\n"
    "v_add_f32 v26, v26, v20\n"
    "v_add_f32 v27, v27, v21\n"
    "ds_read_b128 v[32:35], v14 offset:28672\n"
    ".Lk_s154:\n"
    "v_mfma_f32_16x16x32_bf16 v[96:99], v[176:179], v[240:243], v[96:99]\n"
    "v_mfma_f32_16x16x32_bf16 v[100:103], v[180:183], v[240:243], v[100:103]\n"
    "v_mfma_f32_16x16x32_bf16 v[104:107], v[184:187], v[240:243], v[104:107]\n"
    "v_mfma_f32_16x16x32_bf16 v[108:111], v[188:191], v[240:243], v[108:111]\n"
    "global_load_dwordx4 v[252:255], v2, s[94:95] offset:1024 sc1\n"
    "v_mfma_f32_16x16x32_bf16 v[96:99], v[192:195], v[244:247], v[96:99]\n"
    "v_mfma_f32_16x16x32_bf16 v[100:103], v[196:199], v[244:247], v[100:103]\n"
    "v_mfma_f32_16x16x32_bf16 v[104:107], v[200:203], v[244:247], v[104:107]\n"
    "v_mfma_f32_16x16x32_bf16 v[108:111], v[204:207], v[244:247], v[108:111]\n"
    "global_load_dwordx4 v[44:47], v2, s[94:95] offset:2048 sc1\n"
    "s_cmp_ge_u32 s28, 4\n"
    "s_cbranch_scc1 .Lk_s155\n"
    "s_waitcnt lgkmcnt(0)\n"
    "v_add_f32 v24, v24, v32\n"
    "v_add_f32 v25, v25, v33\n"
    "v_add_f32 v26, v26, v34\n"
    "v_add_f32 v27, v27, v35\n"
    "s_add_u32 s46, s72, 0\n"
    "s_addc_u32 s47, s73, 0\n"
    "v_cvt_pk_bf16_f32 v32, v24, v25\n"
    "v_cvt_pk_bf16_f32 v33, v26, v27\n"
    "global_store_dwordx2 v15, v[32:33], s[46:47] offset:0\n"
    ".Lk_s155:\n"
    "s_branch .Lk_um149\n"
    ".Lk_us148:\n"
    "v_mfma_f32_16x16x32_bf16 v[96:99], v[48:51], v[112:115], 0\n"
    "v_mfma_f32_16x16x32_bf16 v[100:103], v[52:55], v[112:115], 0\n"
    "v_mfma_f32_16x16x32_bf16 v[104:107], v[56:59], v[112:115], 0\n"
    "v_mfma_f32_16x16x32_bf16 v[108:111], v[60:63], v[112:115], 0\n"
    "s_cmp_ge_u32 s28, 4\n"
    "s_cbranch_scc1 .Lk_s156\n"
    "ds_read_b128 v[24:27], v14 offset:0\n"
    "ds_read_b128 v[32:35], v14 offset:4096\n"
    "ds_read_b128 v[36:39], v14 offset:8192\n"
    "ds_read_b128 v[18:21], v14 offset:12288\n"
    ".Lk_s156:\n"
    "v_mfma_f32_16x16x32_bf16 v[96:99], v[64:67], v[116:119], v[96:99]\n"
    "v_mfma_f32_16x16x32_bf16 v[100:103], v[68:71], v[116:119], v[100:103]\n"
    "v_mfma_f32_16x16x32_bf16 v[104:107], v[72:75], v[116:119], v[104:107]\n"
    "v_mfma_f32_16x16x32_bf16 v[108:111], v[76:79], v[116:119], v[108:111]\n"
    "s_cmp_ge_u32 s28, 4\n"
    "s_cbranch_scc1 .Lk_s157\n"
    "ds_read_b64_tr_b16 v[6:7], v31 offset:1024\n"
    "s_waitcnt lgkmcnt(0)\n"
    "v_mfma_f32_16x16x16_bf16 v[96:99], v[16:17], v[6:7], v[96:99]\n"
    "s_nop 5\n"
    ".Lk_s157:\n"
    "v_mfma_f32_16x16x32_bf16 v[96:99], v[80:83], v[120:123], v[96:99]\n"
    "v_mfma_f32_16x16x32_bf16 v[100:103], v[84:87], v[120:123], v[100:103]\n"
    "v_mfma_f32_16x16x32_bf16 v[104:107], v[88:91], v[120:123], v[104:107]\n"
    "v_mfma_f32_16x16x32_bf16 v[108:111], v[92:95], v[120:123], v[108:111]\n"
    "s_cmp_ge_u32 s28, 4\n"
    "s_cbranch_scc1 .Lk_s158\n"
    "s_waitcnt lgkmcnt(0)\n"
    "v_add_f32 v24, v24, v32\n"
    "v_add_f32 v25, v25, v33\n"
    "v_add_f32 v26, v26, v34\n"
    "v_add_f32 v27, v27, v35\n"
    "v_add_f32 v24, v24, v36\n"
    "v_add_f32 v25, v25, v37\n"
    "v_add_f32 v26, v26, v38\n"
    "v_add_f32 v27, v27, v39\n"
    "v_add_f32 v24, v24, v18\n"
    "v_add_f32 v25, v25, v19\n"
    "v_add_f32 v26, v26, v20\n"
    "v_add_f32 v27, v27, v21\n"
    "ds_read_b128 v[32:35], v14 offset:16384\n"
    "ds_read_b128 v[36:39], v14 offset:20480\n"
    "ds_read_b128 v[18:21], v14 offset:24576\n"
    ".Lk_s158:\n"
    "s_add_u32 s52, s22, 13221888\n"
    "s_addc_u32 s53, s23, 0\n"
    "global_load_dwordx2 v[42:43], v30, s[52:53] sc1\n"
    "v_mfma_f32_16x16x32_bf16 v[96:99], v[144:147], v[124:127], v[96:99]\n"
    "v_mfma_f32_16x16x32_bf16 v[100:103], v[148:151], v[124:127], v[100:103]\n"
    "v_mfma_f32_16x16x32_bf16 v[104:107], v[152:155], v[124:127], v[104:107]\n"
    "v_mfma_f32_16x16x32_bf16 v[108:111], v[156:159], v[124:127], v[108:111]\n"
    "v_mfma_f32_16x16x32_bf16 v[96:99], v[160:163], v[236:239], v[96:99]\n"
    "v_mfma_f32_16x16x32_bf16 v[100:103], v[164:167], v[236:239], v[100:103]\n"
    "v_mfma_f32_16x16x32_bf16 v[104:107], v[168:171], v[236:239], v[104:107]\n"
    "v_mfma_f32_16x16x32_bf16 v[108:111], v[172:175], v[236:239], v[108:111]\n"
    "s_cmp_ge_u32 s28, 4\n"
    "s_cbranch_scc1 .Lk_s159\n"
    "s_waitcnt lgkmcnt(0)\n"
    "v_add_f32 v24, v24, v32\n"
    "v_add_f32 v25, v25, v33\n"
    "v_add_f32 v26, v26, v34\n"
    "v_add_f32 v27, v27, v35\n"
    "v_add_f32 v24, v24, v36\n"
    "v_add_f32 v25, v25, v37\n"
    "v_add_f32 v26, v26, v38\n"
    "v_add_f32 v27, v27, v39\n"
    "v_add_f32 v24, v24, v18\n"
    "v_add_f32 v25, v25, v19\n"
    "v_add_f32 v26, v26, v20\n"
    "v_add_f32 v27, v27, v21\n"
    "ds_read_b128 v[32:35], v14 offset:28672\n"
    ".Lk_s159:\n"
    "v_mfma_f32_16x16x32_bf16 v[96:99], v[176:179], v[240:243], v[96:99]\n"
    "v_mfma_f32_16x16x32_bf16 v[100:103], v[180:183], v[240:243], v[100:103]\n"
    "v_mfma_f32_16x16x32_bf16 v[104:107], v[184:187], v[240:243], v[104:107]\n"
    "v_mfma_f32_16x16x32_bf16 v[108:111], v[188:191], v[240:243], v[108:111]\n"
    "v_mfma_f32_16x16x32_bf16 v[96:99], v[192:195], v[244:247], v[96:99]\n"
    "v_mfma_f32_16x16x32_bf16 v[100:103], v[196:199], v[244:247], v[100:103]\n"
    "v_mfma_f32_16x16x32_bf16 v[104:107], v[200:203], v[244:247], v[104:107]\n"
    "v_mfma_f32_16x16x32_bf16 v[108:111], v[204:207], v[244:247], v[108:111]\n"
    "s_cmp_ge_u32 s28, 4\n"
    "s_cbranch_scc1 .Lk_s160\n"
    "s_waitcnt lgkmcnt(0)\n"
    "v_add_f32 v24, v24, v32\n"
    "v_add_f32 v25, v25, v33\n"
    "v_add_f32 v26, v26, v34\n"
    "v_add_f32 v27, v27, v35\n"
    "s_add_u32 s46, s72, 0\n"
    "s_addc_u32 s47, s73, 0\n"
    "v_cvt_pk_bf16_f32 v32, v24, v25\n"
    "v_cvt_pk_bf16_f32 v33, v26, v27\n"
    "global_store_dwordx2 v15, v[32:33], s[46:47] offset:0\n"
    ".Lk_s160:\n"
    "s_waitcnt vmcnt(0)\n"
    "v_cmp_ne_u64 vcc, s[34:35], v[42:43]\n"
    "s_cbranch_vccnz .Lk_r161\n"
    ".Lk_c162:\n"
    "s_cmp_ge_u32 s28, 4\n"
    "s_cbranch_scc1 .Lk_s164\n"
    "s_add_u32 m0, s42, 2048\n"
    "s_add_u32 s92, s68, 917504\n"
    "s_addc_u32 s93, s69, 0\n"
    "global_load_lds_dwordx4 v2, s[92:93] offset:0 sc1\n"
    ".Lk_s164:\n"
    "s_add_u32 s94, s70, 3670016\n"
    "s_addc_u32 s95, s71, 0\n"
    "global_load_dwordx4 v[128:131], v2, s[94:95] offset:0 sc1\n"
    "global_load_dwordx4 v[132:135], v2, s[94:95] offset:1024 sc1\n"
    "global_load_dwordx4 v[136:139], v2, s[94:95] offset:2048 sc1\n"
    "global_load_dwordx4 v[140:143], v2, s[94:95] offset:3072 sc1\n"
    "s_add_u32 s94, s70, 3674112\n"
    "s_addc_u32 s95, s71, 0\n"
    "global_load_dwordx4 v[248:251], v2, s[94:95] offset:0 sc1\n"
    "global_load_dwordx4 v[252:255], v2, s[94:95] offset:1024 sc1\n"
    "global_load_dwordx4 v[44:47], v2, s[94:95] offset:2048 sc1\n"
    "s_add_u32 s52, s22, 13254656\n"
    "s_addc_u32 s53, s23, 0\n"
    "global_load_dwordx2 v[40:41], v30, s[52:53] sc1\n"
    ".Lk_um149:\n"
    "v_add_u32 v22, s59, v10\n"
    "s_nop 1\n"
    "ds_write_b128 v22, v[96:99]\n"
    "v_add_u32 v22, s59, v11\n"
    "ds_write_b128 v22, v[100:103]\n"
    "v_add_u32 v22, s59, v12\n"
    "ds_write_b128 v22, v[104:107]\n"
    "v_add_u32 v22, s59, v13\n"
    "ds_write_b128 v22, v[108:111]\n"
    "s_waitcnt lgkmcnt(0)\n"
    "s_barrier\n"
    "s_waitcnt vmcnt(0)\n"
    "v_cmp_ne_u64 vcc, s[34:35], v[40:41]\n"
    "s_cbranch_vccnz .Lk_us165\n"
    "v_mfma_f32_16x16x32_bf16 v[96:99], v[48:51], v[128:131], 0\n"
    "v_mfma_f32_16x16x32_bf16 v[100:103], v[52:55], v[128:131], 0\n"
    "v_mfma_f32_16x16x32_bf16 v[104:107], v[56:59], v[128:131], 0\n"
    "v_mfma_f32_16x16x32_bf16 v[108:111], v[60:63], v[128:131], 0\n"
    "s_cmp_ge_u32 s28, 4\n"
    "s_cbranch_scc1 .Lk_s167\n"
    "s_add_u32 m0, s42, 3072\n"
    "s_add_u32 s92, s68, 1376256\n"
    "s_addc_u32 s93, s69, 0\n"
    "global_load_lds_dwordx4 v2, s[92:93] offset:0 sc1\n"
    ".Lk_s167:\n"
    "s_add_u32 s94, s70, 5505024\n"
    "s_addc_u32 s95, s71, 0\n"
    "global_load_dwordx4 v[208:211], v2, s[94:95] offset:0 sc1\n"
    "s_cmp_lt_u32 s28, 4\n"
    "s_cbranch_scc1 .Lk_s168\n"
    "ds_read_b128 v[24:27], v14 offset:0\n"
    "ds_read_b128 v[32:35], v14 offset:4096\n"
    "ds_read_b128 v[36:39], v14 offset:8192\n"
    "ds_read_b128 v[18:21], v14 offset:12288\n"
    ".Lk_s168:\n"
    "v_mfma_f32_16x16x32_bf16 v[96:99], v[64:67], v[132:135], v[96:99]\n"
    "v_mfma_f32_16x16x32_bf16 v[100:103], v[68:71], v[132:135], v[100:103]\n"
    "v_mfma_f32_16x16x32_bf16 v[104:107], v[72:75], v[132:135], v[104:107]\n"
    "v_mfma_f32_16x16x32_bf16 v[108:111], v[76:79], v[132:135], v[108:111]\n"
    "global_load_dwordx4 v[212:215], v2, s[94:95] offset:1024 sc1\n"
    "s_cmp_ge_u32 s28, 4\n"
    "s_cbranch_scc1 .Lk_s169\n"
    "ds_read_b64_tr_b16 v[6:7], v31 offset:2048\n"
    "s_waitcnt lgkmcnt(0)\n"
    "v_mfma_f32_16x16x16_bf16 v[96:99], v[16:17], v[6:7], v[96:99]\n"
    "s_nop 5\n"
    ".Lk_s169:\n"
    "v_mfma_f32_16x16x32_bf16 v[96:99], v[80:83], v[136:139], v[96:99]\n"
    "v_mfma_f32_16x16x32_bf16 v[100:103], v[84:87], v[136:139], v[100:103]\n"
    "v_mfma_f32_16x16x32_bf16 v[104:107], v[88:91], v[136:139], v[104:107]\n"
    "v_mfma_f32_16x16x32_bf16 v[108:111], v[92:95], v[136:139], v[108:111]\n"
    "global_load_dwordx4 v[216:219], v2, s[94:95] offset:2048 sc1\n"
    "s_cmp_lt_u32 s28, 4\n"
    "s_cbranch_scc1 .Lk_s170\n"
    "s_waitcnt lgkmcnt(0)\n"
    "v_add_f32 v24, v24, v32\n"
    "v_add_f32 v25, v25, v33\n"
    "v_add_f32 v26, v26, v34\n"
    "v_add_f32 v27, v27, v35\n"
    "v_add_f32 v24, v24, v36\n"
    "v_add_f32 v25, v25, v37\n"
    "v_add_f32 v26, v26, v38\n"
    "v_add_f32 v27, v27, v39\n"
    "v_add_f32 v24, v24, v18\n"
    "v_add_f32 v25, v25, v19\n"
    "v_add_f32 v26, v26, v20\n"
    "v_add_f32 v27, v27, v21\n"
    "ds_read_b128 v[32:35], v14 offset:16384\n"
    "ds_read_b128 v[36:39], v14 offset:20480\n"
    "ds_read_b128 v[18:21], v14 offset:24576\n"
    ".Lk_s170:\n"
    "v_mfma_f32_16x16x32_bf16 v[96:99], v[144:147], v[140:143], v[96:99]\n"
    "v_mfma_f32_16x16x32_bf16 v[100:103], v[148:151], v[140:143], v[100:103]\n"
    "v_mfma_f32_16x16x32_bf16 v[104:107], v[152:155], v[140:143], v[104:107]\n"
    "v_mfma_f32_16x16x32_bf16 v[108:111], v[156:159], v[140:143], v[108:111]\n"
    "global_load_dwordx4 v[220:223], v2, s[94:95] offset:3072 sc1\n"
    "v_mfma_f32_16x16x32_bf16 v[96:99], v[160:163], v[248:251], v[96:99]\n"
    "v_mfma_f32_16x16x32_bf16 v[100:103], v[164:167], v[248:251], v[100:103]\n"
    "v_mfma_f32_16x16x32_bf16 v[104:107], v[168:171], v[248:251], v[104:107]\n"
    "v_mfma_f32_16x16x32_bf16 v[108:111], v[172:175], v[248:251], v[108:111]\n"
    "s_add_u32 s94, s70, 5509120\n"
    "s_addc_u32 s95, s71, 0\n"
    "global_load_dwordx4 v[224:227], v2, s[94:95] offset:0 sc1\n"
    "s_cmp_lt_u32 s28, 4\n"
    "s_cbranch_scc1 .Lk_s171\n"
    "s_waitcnt lgkmcnt(0)\n"
    "v_add_f32 v24, v24, v32\n"
    "v_add_f32 v25, v25, v33\n"
    "v_add_f32 v26, v26, v34\n"
    "v_add_f32 v27, v27, v35\n"
    "v_add_f32 v24, v24, v36\n"
    "v_add_f32 v25, v25, v37\n"
    "v_add_f32 v26, v26, v38\n"
    "v_add_f32 v27, v27, v39\n"
    "v_add_f32 v24, v24, v18\n"
    "v_add_f32 v25, v25, v19\n"
    "v_add_f32 v26, v26, v20\n"
    "v_add_f32 v27, v27, v21\n"
    "ds_read_b128 v[32:35], v14 offset:28672\n"
    ".Lk_s171:\n"
    "v_mfma_f32_16x16x32_bf16 v[96:99], v[176:179], v[252:255], v[96:99]\n"
    "v_mfma_f32_16x16x32_bf16 v[100:103], v[180:183], v[252:255], v[100:103]\n"
    "v_mfma_f32_16x16x32_bf16 v[104:107], v[184:187], v[252:255], v[104:107]\n"
    "v_mfma_f32_16x16x32_bf16 v[108:111], v[188:191], v[252:255], v[108:111]\n"
    "global_load_dwordx4 v[228:231], v2, s[94:95] offset:1024 sc1\n"
    "v_mfma_f32_16x16x32_bf16 v[96:99], v[192:195], v[44:47], v[96:99]\n"
    "v_mfma_f32_16x16x32_bf16 v[100:103], v[196:199], v[44:47], v[100:103]\n"
    "v_mfma_f32_16x16x32_bf16 v[104:107], v[200:203], v[44:47], v[104:107]\n"
    "v_mfma_f32_16x16x32_bf16 v[108:111], v[204:207], v[44:47], v[108:111]\n"
    "global_load_dwordx4 v[232:235], v2, s[94:95] offset:2048 sc1\n"
    "s_cmp_lt_u32 s28, 4\n"
    "s_cbranch_scc1 .Lk_s172\n"
    "s_waitcnt lgkmcnt(0)\n"
    "v_add_f32 v24, v24, v32\n"
    "v_add_f32 v25, v25, v33\n"
    "v_add_f32 v26, v26, v34\n"
    "v_add_f32 v27, v27, v35\n"
    "s_add_u32 s46, s72, 458752\n"
    "s_addc_u32 s47, s73, 0\n"
    "v_cvt_pk_bf16_f32 v32, v24, v25\n"
    "v_cvt_pk_bf16_f32 v33, v26, v27\n"
    "global_store_dwordx2 v15, v[32:33], s[46:47] offset:0\n"
    ".Lk_s172:\n"
    "s_branch .Lk_um166\n"
    ".Lk_us165:\n"
    "v_mfma_f32_16x16x32_bf16 v[96:99], v[48:51], v[128:131], 0\n"
    "v_mfma_f32_16x16x32_bf16 v[100:103], v[52:55], v[128:131], 0\n"
    "v_mfma_f32_16x16x32_bf16 v[104:107], v[56:59], v[128:131], 0\n"
    "v_mfma_f32_16x16x32_bf16 v[108:111], v[60:63], v[128:131], 0\n"
    "s_cmp_lt_u32 s28, 4\n"
    "s_cbranch_scc1 .Lk_s173\n"
    "ds_read_b128 v[24:27], v14 offset:0\n"
    "ds_read_b128 v[32:35], v14 offset:4096\n"
    "ds_read_b128 v[36:39], v14 offset:8192\n"
    "ds_read_b128 v[18:21], v14 offset:12288\n"
    ".Lk_s173:\n"
    "v_mfma_f32_16x16x32_bf16 v[96:99], v[64:67], v[132:135], v[96:99]\n"
    "v_mfma_f32_16x16x32_bf16 v[100:103], v[68:71], v[132:135], v[100:103]\n"
    "v_mfma_f32_16x16x32_bf16 v[104:107], v[72:75], v[132:135], v[104:107]\n"
    "v_mfma_f32_16x16x32_bf16 v[108:111], v[76:79], v[132:135], v[108:111]\n"
    "s_cmp_ge_u32 s28, 4\n"
    "s_cbranch_scc1 .Lk_s174\n"
    "ds_read_b64_tr_b16 v[6:7], v31 offset:2048\n"
    "s_waitcnt lgkmcnt(0)\n"
    "v_mfma_f32_16x16x16_bf16 v[96:99], v[16:17], v[6:7], v[96:99]\n"
    "s_nop 5\n"
    ".Lk_s174:\n"
    "v_mfma_f32_16x16x32_bf16 v[96:99], v[80:83], v[136:139], v[96:99]\n"
    "v_mfma_f32_16x16x32_bf16 v[100:103], v[84:87], v[136:139], v[100:103]\n"
    "v_mfma_f32_16x16x32_bf16 v[104:107], v[88:91], v[136:139], v[104:107]\n"
    "v_mfma_f32_16x16x32_bf16 v[108:111], v[92:95], v[136:139], v[108:111]\n"
    "s_cmp_lt_u32 s28, 4\n"
    "s_cbranch_scc1 .Lk_s175\n"
    "s_waitcnt lgkmcnt(0)\n"
    "v_add_f32 v24, v24, v32\n"
    "v_add_f32 v25, v25, v33\n"
    "v_add_f32 v26, v26, v34\n"
    "v_add_f32 v27, v27, v35\n"
    "v_add_f32 v24, v24, v36\n"
    "v_add_f32 v25, v25, v37\n"
    "v_add_f32 v26, v26, v38\n"
    "v_add_f32 v27, v27, v39\n"
    "v_add_f32 v24, v24, v18\n"
    "v_add_f32 v25, v25, v19\n"
    "v_add_f32 v26, v26, v20\n"
    "v_add_f32 v27, v27, v21\n"
    "ds_read_b128 v[32:35], v14 offset:16384\n"
    "ds_read_b128 v[36:39], v14 offset:20480\n"
    "ds_read_b128 v[18:21], v14 offset:24576\n"
    ".Lk_s175:\n"
    "s_add_u32 s52, s22, 13254656\n"
    "s_addc_u32 s53, s23, 0\n"
    "global_load_dwordx2 v[40:41], v30, s[52:53] sc1\n"
    "v_mfma_f32_16x16x32_bf16 v[96:99], v[144:147], v[140:143], v[96:99]\n"
    "v_mfma_f32_16x16x32_bf16 v[100:103], v[148:151], v[140:143], v[100:103]\n"
    "v_mfma_f32_16x16x32_bf16 v[104:107], v[152:155], v[140:143], v[104:107]\n"
    "v_mfma_f32_16x16x32_bf16 v[108:111], v[156:159], v[140:143], v[108:111]\n"
    "v_mfma_f32_16x16x32_bf16 v[96:99], v[160:163], v[248:251], v[96:99]\n"
    "v_mfma_f32_16x16x32_bf16 v[100:103], v[164:167], v[248:251], v[100:103]\n"
    "v_mfma_f32_16x16x32_bf16 v[104:107], v[168:171], v[248:251], v[104:107]\n"
    "v_mfma_f32_16x16x32_bf16 v[108:111], v[172:175], v[248:251], v[108:111]\n"
    "s_cmp_lt_u32 s28, 4\n"
    "s_cbranch_scc1 .Lk_s176\n"
    "s_waitcnt lgkmcnt(0)\n"
    "v_add_f32 v24, v24, v32\n"
    "v_add_f32 v25, v25, v33\n"
    "v_add_f32 v26, v26, v34\n"
    "v_add_f32 v27, v27, v35\n"
    "v_add_f32 v24, v24, v36\n"
    "v_add_f32 v25, v25, v37\n"
    "v_add_f32 v26, v26, v38\n"
    "v_add_f32 v27, v27, v39\n"
    "v_add_f32 v24, v24, v18\n"
    "v_add_f32 v25, v25, v19\n"
    "v_add_f32 v26, v26, v20\n"
    "v_add_f32 v27, v27, v21\n"
    "ds_read_b128 v[32:35], v14 offset:28672\n"
    ".Lk_s176:\n"
    "v_mfma_f32_16x16x32_bf16 v[96:99], v[176:179], v[252:255], v[96:99]\n"
    "v_mfma_f32_16x16x32_bf16 v[100:103], v[180:183], v[252:255], v[100:103]\n"
    "v_mfma_f32_16x16x32_bf16 v[104:107], v[184:187], v[252:255], v[104:107]\n"
    "v_mfma_f32_16x16x32_bf16 v[108:111], v[188:191], v[252:255], v[108:111]\n"
    "v_mfma_f32_16x16x32_bf16 v[96:99], v[192:195], v[44:47], v[96:99]\n"
    "v_mfma_f32_16x16x32_bf16 v[100:103], v[196:199], v[44:47], v[100:103]\n"
    "v_mfma_f32_16x16x32_bf16 v[104:107], v[200:203], v[44:47], v[104:107]\n"
    "v_mfma_f32_16x16x32_bf16 v[108:111], v[204:207], v[44:47], v[108:111]\n"
    "s_cmp_lt_u32 s28, 4\n"
    "s_cbranch_scc1 .Lk_s177\n"
    "s_waitcnt lgkmcnt(0)\n"
    "v_add_f32 v24, v24, v32\n"
    "v_add_f32 v25, v25, v33\n"
    "v_add_f32 v26, v26, v34\n"
    "v_add_f32 v27, v27, v35\n"
    "s_add_u32 s46, s72, 458752\n"
    "s_addc_u32 s47, s73, 0\n"
    "v_cvt_pk_bf16_f32 v32, v24, v25\n"
    "v_cvt_pk_bf16_f32 v33, v26, v27\n"
    "global_store_dwordx2 v15, v[32:33], s[46:47] offset:0\n"
    ".Lk_s177:\n"
    "s_waitcnt vmcnt(0)\n"
    "v_cmp_ne_u64 vcc, s[34:35], v[40:41]\n"
    "s_cbranch_vccnz .Lk_r178\n"
    ".Lk_c179:\n"
    "s_cmp_ge_u32 s28, 4\n"
    "s_cbranch_scc1 .Lk_s181\n"
    "s_add_u32 m0, s42, 3072\n"
    "s_add_u32 s92, s68, 1376256\n"
    "s_addc_u32 s93, s69, 0\n"
    "global_load_lds_dwordx4 v2, s[92:93] offset:0 sc1\n"
    ".Lk_s181:\n"
    "s_add_u32 s94, s70, 5505024\n"
    "s_addc_u32 s95, s71, 0\n"
    "global_load_dwordx4 v[208:211], v2, s[94:95] offset:0 sc1\n"
    "global_load_dwordx4 v[212:215], v2, s[94:95] offset:1024 sc1\n"
    "global_load_dwordx4 v[216:219], v2, s[94:95] offset:2048 sc1\n"
    "global_load_dwordx4 v[220:223], v2, s[94:95] offset:3072 sc1\n"
    "s_add_u32 s94, s70, 5509120\n"
    "s_addc_u32 s95, s71, 0\n"
    "global_load_dwordx4 v[224:227], v2, s[94:95] offset:0 sc1\n"
    "global_load_dwordx4 v[228:231], v2, s[94:95] offset:1024 sc1\n"
    "global_load_dwordx4 v[232:235], v2, s[94:95] offset:2048 sc1\n"
    ".Lk_um166:\n"
    "s_nop 2\n"
    "ds_write_b128 v10, v[96:99]\n"
    "ds_write_b128 v11, v[100:103]\n"
    "ds_write_b128 v12, v[104:107]\n"
    "ds_write_b128 v13, v[108:111]\n"
    "s_barrier\n"
    "s_lshr_b32 s55, s28, 2\n"
    "s_add_u32 s55, s55, 2\n"
    "s_mul_i32 s55, s55, 458752\n"
    "s_add_u32 s46, s72, s55\n"
    "s_addc_u32 s47, s73, 0\n"
    "s_waitcnt vmcnt(6)\n"
    "v_mfma_f32_16x16x32_bf16 v[96:99], v[48:51], v[208:211], 0\n"
    "v_mfma_f32_16x16x32_bf16 v[100:103], v[52:55], v[208:211], 0\n"
    "v_mfma_f32_16x16x32_bf16 v[104:107], v[56:59], v[208:211], 0\n"
    "v_mfma_f32_16x16x32_bf16 v[108:111], v[60:63], v[208:211], 0\n"
    "s_waitcnt vmcnt(5)\n"
    "v_mfma_f32_16x16x32_bf16 v[96:99], v[64:67], v[212:215], v[96:99]\n"
    "v_mfma_f32_16x16x32_bf16 v[100:103], v[68:71], v[212:215], v[100:103]\n"
    "v_mfma_f32_16x16x32_bf16 v[104:107], v[72:75], v[212:215], v[104:107]\n"
    "v_mfma_f32_16x16x32_bf16 v[108:111], v[76:79], v[212:215], v[108:111]\n"
    "s_cmp_ge_u32 s28, 4\n"
    "s_cbranch_scc1 .Lk_s182\n"
    "ds_read_b64_tr_b16 v[6:7], v31 offset:3072\n"
    "s_waitcnt lgkmcnt(0)\n"
    "v_mfma_f32_16x16x16_bf16 v[96:99], v[16:17], v[6:7], v[96:99]\n"
    "s_nop 5\n"
    ".Lk_s182:\n"
    "s_waitcnt vmcnt(4)\n"
    "v_mfma_f32_16x16x32_bf16 v[96:99], v[80:83], v[216:219], v[96:99]\n"
    "v_mfma_f32_16x16x32_bf16 v[100:103], v[84:87], v[216:219], v[100:103]\n"
    "v_mfma_f32_16x16x32_bf16 v[104:107], v[88:91], v[216:219], v[104:107]\n"
    "v_mfma_f32_16x16x32_bf16 v[108:111], v[92:95], v[216:219], v[108:111]\n"
    "s_waitcnt vmcnt(3)\n"
    "v_mfma_f32_16x16x32_bf16 v[96:99], v[144:147], v[220:223], v[96:99]\n"
    "v_mfma_f32_16x16x32_bf16 v[100:103], v[148:151], v[220:223], v[100:103]\n"
    "v_mfma_f32_16x16x32_bf16 v[104:107], v[152:155], v[220:223], v[104:107]\n"
    "v_mfma_f32_16x16x32_bf16 v[108:111], v[156:159], v[220:223], v[108:111]\n"
    "s_waitcnt vmcnt(2)\n"
    "v_mfma_f32_16x16x32_bf16 v[96:99], v[160:163], v[224:227], v[96:99]\n"
    "v_mfma_f32_16x16x32_bf16 v[100:103], v[164:167], v[224:227], v[100:103]\n"
    "v_mfma_f32_16x16x32_bf16 v[104:107], v[168:171], v[224:227], v[104:107]\n"
    "v_mfma_f32_16x16x32_bf16 v[108:111], v[172:175], v[224:227], v[108:111]\n"
    "s_waitcnt vmcnt(1)\n"
    "v_mfma_f32_16x16x32_bf16 v[96:99], v[176:179], v[228:231], v[96:99]\n"
    "v_mfma_f32_16x16x32_bf16 v[100:103], v[180:183], v[228:231], v[100:103]\n"
    "v_mfma_f32_16x16x32_bf16 v[104:107], v[184:187], v[228:231], v[104:107]\n"
    "v_mfma_f32_16x16x32_bf16 v[108:111], v[188:191], v[228:231], v[108:111]\n"
    "s_waitcnt vmcnt(0)\n"
    "v_mfma_f32_16x16x32_bf16 v[96:99], v[192:195], v[232:235], v[96:99]\n"
    "v_mfma_f32_16x16x32_bf16 v[100:103], v[196:199], v[232:235], v[100:103]\n"
    "v_mfma_f32_16x16x32_bf16 v[104:107], v[200:203], v[232:235], v[104:107]\n"
    "v_mfma_f32_16x16x32_bf16 v[108:111], v[204:207], v[232:235], v[108:111]\n"
    "v_add_u32 v22, s59, v10\n"
    "s_nop 3\n"
    "ds_write_b128 v22, v[96:99]\n"
    "v_add_u32 v22, s59, v11\n"
    "ds_write_b128 v22, v[100:103]\n"
    "v_add_u32 v22, s59, v12\n"
    "ds_write_b128 v22, v[104:107]\n"
    "v_add_u32 v22, s59, v13\n"
    "ds_write_b128 v22, v[108:111]\n"
    "s_waitcnt lgkmcnt(0)\n"
    "s_barrier\n"
    "ds_read_b128 v[144:147], v14 offset:0\n"
    "ds_read_b128 v[148:151], v14 offset:4096\n"
    "ds_read_b128 v[152:155], v14 offset:8192\n"
    "ds_read_b128 v[156:159], v14 offset:12288\n"
    "ds_read_b128 v[160:163], v14 offset:16384\n"
    "ds_read_b128 v[164:167], v14 offset:20480\n"
    "ds_read_b128 v[168:171], v14 offset:24576\n"
    "ds_read_b128 v[172:175], v14 offset:28672\n"
    "s_waitcnt lgkmcnt(6)\n"
    "v_pk_add_f32 v[144:145], v[144:145], v[148:149]\n"
    "v_pk_add_f32 v[146:147], v[146:147], v[150:151]\n"
    "s_waitcnt lgkmcnt(4)\n"
    "v_pk_add_f32 v[152:153], v[152:153], v[156:157]\n"
    "v_pk_add_f32 v[154:155], v[154:155], v[158:159]\n"
    "s_waitcnt lgkmcnt(2)\n"
    "v_pk_add_f32 v[160:161], v[160:161], v[164:165]\n"
    "v_pk_add_f32 v[162:163], v[162:163], v[166:167]\n"
    "s_waitcnt lgkmcnt(0)\n"
    "v_pk_add_f32 v[168:169], v[168:169], v[172:173]\n"
    "v_pk_add_f32 v[170:171], v[170:171], v[174:175]\n"
    "v_pk_add_f32 v[144:145], v[144:145], v[152:153]\n"
    "v_pk_add_f32 v[146:147], v[146:147], v[154:155]\n"
    "v_pk_add_f32 v[160:161], v[160:161], v[168:169]\n"
    "v_pk_add_f32 v[162:163], v[162:163], v[170:171]\n"
    "v_pk_add_f32 v[144:145], v[144:145], v[160:161]\n"
    "v_pk_add_f32 v[146:147], v[146:147], v[162:163]\n"
    "v_cvt_pk_bf16_f32 v148, v144, v145\n"
    "v_cvt_pk_bf16_f32 v149, v146, v147\n"
    "global_store_dwordx2 v15, v[148:149], s[46:47] offset:0\n"
    "s_or_b32 s55, %2, s28\n"
    "s_cbranch_scc1 .Lk_fin\n"
    "s_add_u32 s46, s22, 18163712\n"
    "s_addc_u32 s47, s23, 0\n"
    "s_mov_b64 exec, 1\n"
    "global_store_dwordx2 v3, v[4:5], s[46:47] sc1\n"
    "s_mov_b64 exec, -1\n"
    ".Lk_fin:\n"
    "s_endpgm\n"
    ".Lk_Bh:\n"
    "s_and_b32 s55, s26, 3\n"
    "s_and_b32 s56, s27, 3\n"
    "s_lshl2_add_u32 s30, s55, s56\n"
    "s_lshr_b32 s41, s26, 2\n"
    "s_mul_i32 s31, s41, 7\n"
    "s_lshr_b32 s56, s27, 2\n"
    "s_add_u32 s31, s31, s56\n"
    "s_lshl_b32 s55, s55, 1\n"
    "s_add_u32 s55, s55, s41\n"
    "s_lshl_b32 s55, s55, 12\n"
    "s_add_u32 s55, s55, 13025280\n"
    "s_add_u32 s78, s22, s55\n"
    "s_addc_u32 s79, s23, 0\n"
    "s_mul_i32 s55, s30, 28672\n"
    "s_add_u32 s55, s55, 3719168\n"
    "s_add_u32 s80, s22, s55\n"
    "s_addc_u32 s81, s23, 0\n"
    "s_lshl_b32 s55, s41, 14\n"
    "s_lshl_b32 s56, s30, 8\n"
    "s_add_u32 s55, s55, s56\n"
    "s_add_u32 s55, s55, 3686400\n"
    "s_add_u32 s82, s22, s55\n"
    "s_addc_u32 s83, s23, 0\n"
    "s_memrealtime s[60:61]\n"
    "s_min_u32 s55, s29, 223\n"
    "s_lshl_b32 s55, s55, 2\n"
    "s_load_dword s36, s[14:15], s55\n"
    "s_getpc_b64 s[48:49]\n"
    ".Lk_pc:\n"
    "s_sub_u32 s48, s48, .Lk_pc-.Lk_begin\n"
    "s_subb_u32 s49, s49, 0\n"
    "s_lshl_b32 s55, s91, 6\n"
    "v_add_lshl_u32 v6, v1, s55, 7\n"
    "v_min_u32 v6, .Lk_end-.Lk_begin-4, v6\n"
    "global_load_dword v7, v6, s[48:49]\n"
    "s_waitcnt vmcnt(0) lgkmcnt(0)\n"
    "s_mov_b32 s54, s60\n"
    "v_readfirstlane_b32 s34, v64\n"
    "v_readfirstlane_b32 s35, v65\n"
    "s_add_u32 s34, s34, 0x7F4A7C15\n"
    "s_addc_u32 s35, s35, 0x9E3779B9\n"
    "v_mov_b32 v4, s34\n"
    "v_mov_b32 v5, s35\n"
    "v_lshlrev_b32 v66, 16, v32\n"
    "v_and_b32 v67, 0xffff0000, v32\n"
    "v_lshlrev_b32 v68, 16, v33\n"
    "v_and_b32 v69, 0xffff0000, v33\n"
    "v_lshlrev_b32 v70, 16, v34\n"
    "v_and_b32 v71, 0xffff0000, v34\n"
    "v_lshlrev_b32 v72, 16, v35\n"
    "v_and_b32 v73, 0xffff0000, v35\n"
    "v_lshlrev_b32 v74, 16, v36\n"
    "v_and_b32 v75, 0xffff0000, v36\n"
    "v_lshlrev_b32 v76, 16, v37\n"
    "v_and_b32 v77, 0xffff0000, v37\n"
    "v_lshlrev_b32 v78, 16, v38\n"
    "v_and_b32 v79, 0xffff0000, v38\n"
    "v_lshlrev_b32 v80, 16, v39\n"
    "v_and_b32 v81, 0xffff0000, v39\n"
    "v_pk_add_f32 v[84:85], v[66:67], v[68:69]\n"
    "v_pk_add_f32 v[86:87], v[70:71], v[72:73]\n"
    "v_pk_add_f32 v[88:89], v[74:75], v[76:77]\n"
    "v_pk_add_f32 v[90:91], v[78:79], v[80:81]\n"
    "v_pk_add_f32 v[84:85], v[84:85], v[86:87]\n"
    "v_pk_add_f32 v[88:89], v[88:89], v[90:91]\n"
    "v_add_f32 v84, v84, v85\n"
    "v_add_f32 v88, v88, v89\n"
    "v_cndmask_b32 v84, 0, v84, s[62:63]\n"
    "v_cndmask_b32 v88, 0, v88, s[62:63]\n"
    "s_nop 0\n"
    "v_add_f32_dpp v84, v84, v84 quad_perm:[1,0,3,2] row_mask:0xf bank_mask:0xf\n"
    "v_add_f32_dpp v88, v88, v88 quad_perm:[1,0,3,2] row_mask:0xf bank_mask:0xf\n"
    "s_nop 0\n"
    "v_add_f32_dpp v84, v84, v84 quad_perm:[2,3,0,1] row_mask:0xf bank_mask:0xf\n"
    "v_add_f32_dpp v88, v88, v88 quad_perm:[2,3,0,1] row_mask:0xf bank_mask:0xf\n"
    "s_nop 0\n"
    "v_add_f32_dpp v84, v84, v84 row_half_mirror row_mask:0xf bank_mask:0xf\n"
    "v_add_f32_dpp v88, v88, v88 row_half_mirror row_mask:0xf bank_mask:0xf\n"
    "s_nop 0\n"
    "v_add_f32_dpp v84, v84, v84 row_mirror row_mask:0xf bank_mask:0xf\n"
    "v_add_f32_dpp v88, v88, v88 row_mirror row_mask:0xf bank_mask:0xf\n"
    "v_mov_b32 v85, v84\n"
    "v_mov_b32 v89, v88\n"
    "s_nop 0\n"
    "v_permlane16_swap_b32 v84, v85\n"
    "v_permlane16_swap_b32 v88, v89\n"
    "v_add_f32 v84, v84, v85\n"
    "v_add_f32 v88, v88, v89\n"
    "v_mov_b32 v85, v84\n"
    "v_mov_b32 v89, v88\n"
    "s_nop 0\n"
    "v_permlane32_swap_b32 v84, v85\n"
    "v_permlane32_swap_b32 v88, v89\n"
    "v_add_f32 v84, v84, v85\n"
    "v_add_f32 v88, v88, v89\n"
    "v_mul_f32 v84, 0x3b124925, v84\n"
    "v_mul_f32 v88, 0x3b124925, v88\n"
    "v_sub_f32 v66, v66, v84\n"
    "v_sub_f32 v74, v74, v88\n"
    "v_sub_f32 v67, v67, v84\n"
    "v_sub_f32 v75, v75, v88\n"
    "v_sub_f32 v68, v68, v84\n"
    "v_sub_f32 v76, v76, v88\n"
    "v_sub_f32 v69, v69, v84\n"
    "v_sub_f32 v77, v77, v88\n"
    "v_sub_f32 v70, v70, v84\n"
    "v_sub_f32 v78, v78, v88\n"
    "v_sub_f32 v71, v71, v84\n"
    "v_sub_f32 v79, v79, v88\n"
    "v_sub_f32 v72, v72, v84\n"
    "v_sub_f32 v80, v80, v88\n"
    "v_sub_f32 v73, v73, v84\n"
    "v_sub_f32 v81, v81, v88\n"
    "v_mul_f32 v86, v66, v66\n"
    "v_mul_f32 v90, v74, v74\n"
    "v_fmac_f32 v86, v67, v67\n"
    "v_fmac_f32 v90, v75, v75\n"
    "v_fmac_f32 v86, v68, v68\n"
    "v_fmac_f32 v90, v76, v76\n"
    "v_fmac_f32 v86, v69, v69\n"
    "v_fmac_f32 v90, v77, v77\n"
    "v_fmac_f32 v86, v70, v70\n"
    "v_fmac_f32 v90, v78, v78\n"
    "v_fmac_f32 v86, v71, v71\n"
    "v_fmac_f32 v90, v79, v79\n"
    "v_fmac_f32 v86, v72, v72\n"
    "v_fmac_f32 v90, v80, v80\n"
    "v_fmac_f32 v86, v73, v73\n"
    "v_fmac_f32 v90, v81, v81\n"
    "v_cndmask_b32 v86, 0, v86, s[62:63]\n"
    "v_cndmask_b32 v90, 0, v90, s[62:63]\n"
    "s_nop 0\n"
    "v_add_f32_dpp v86, v86, v86 quad_perm:[1,0,3,2] row_mask:0xf bank_mask:0xf\n"
    "v_add_f32_dpp v90, v90, v90 quad_perm:[1,0,3,2] row_mask:0xf bank_mask:0xf\n"
    "s_nop 0\n"
    "v_add_f32_dpp v86, v86, v86 quad_perm:[2,3,0,1] row_mask:0xf bank_mask:0xf\n"
    "v_add_f32_dpp v90, v90, v90 quad_perm:[2,3,0,1] row_mask:0xf bank_mask:0xf\n"
    "s_nop 0\n"
    "v_add_f32_dpp v86, v86, v86 row_half_mirror row_mask:0xf bank_mask:0xf\n"
    "v_add_f32_dpp v90, v90, v90 row_half_mirror row_mask:0xf bank_mask:0xf\n"
    "s_nop 0\n"
    "v_add_f32_dpp v86, v86, v86 row_mirror row_mask:0xf bank_mask:0xf\n"
    "v_add_f32_dpp v90, v90, v90 row_mirror row_mask:0xf bank_mask:0xf\n"
    "v_mov_b32 v87, v86\n"
    "v_mov_b32 v91, v90\n"
    "s_nop 0\n"
    "v_permlane16_swap_b32 v86, v87\n"
    "v_permlane16_swap_b32 v90, v91\n"
    "v_add_f32 v86, v86, v87\n"
    "v_add_f32 v90, v90, v91\n"
    "v_mov_b32 v87, v86\n"
    "v_mov_b32 v91, v90\n"
    "s_nop 0\n"
    "v_permlane32_swap_b32 v86, v87\n"
    "v_permlane32_swap_b32 v90, v91\n"
    "v_add_f32 v86, v86, v87\n"
    "v_add_f32 v90, v90, v91\n"
    "v_mul_f32 v86, 0x3b124925, v86\n"
    "v_mul_f32 v90, 0x3b124925, v90\n"
    "v_cvt_pk_bf16_f32 v86, v86, v86\n"
    "v_and_b32 v86, 0xffff0000, v86\n"
    "v_add_f32 v86, 0x3727c5ac, v86\n"
    "v_cvt_pk_bf16_f32 v86, v86, v86\n"
    "v_and_b32 v86, 0xffff0000, v86\n"
    "v_sqrt_f32 v86, v86\n"
    "s_nop 0\n"
    "v_cvt_pk_bf16_f32 v32, v84, v86\n"
    "v_mov_b32 v33, s34\n"
    "v_cvt_pk_bf16_f32 v90, v90, v90\n"
    "v_and_b32 v90, 0xffff0000, v90\n"
    "v_add_f32 v90, 0x3727c5ac, v90\n"
    "v_cvt_pk_bf16_f32 v90, v90, v90\n"
    "v_and_b32 v90, 0xffff0000, v90\n"
    "v_sqrt_f32 v90, v90\n"
    "s_nop 0\n"
    "v_cvt_pk_bf16_f32 v34, v88, v90\n"
    "v_mov_b32 v35, s34\n"
    "s_lshl_b32 s55, s90, 6\n"
    "s_lshl4_add_u32 s55, s91, s55\n"
    "s_add_u32 s55, s55, 3670016\n"
    "s_add_u32 s46, s22, s55\n"
    "s_addc_u32 s47, s23, 0\n"
    "s_mov_b64 exec, 1\n"
    "global_store_dwordx2 v3, v[32:33], s[46:47] offset:0 sc1\n"
    "global_store_dwordx2 v3, v[34:35], s[46:47] offset:8 sc1\n"
    "s_mov_b64 exec, -1\n"
    "s_branch .Lk_helper\n"
    ".Lk_helper:\n"
    "s_waitcnt lgkmcnt(0)\n"
    "v_min_u32 v10, 27, v1\n"
    "s_mul_i32 s55, s28, 28\n"
    "v_add_lshl_u32 v7, v10, s55, 4\n"
    "s_mul_i32 s64, s28, 3584\n"
    "s_add_u32 s80, s80, s64\n"
    "s_addc_u32 s81, s81, 0\n"
    "s_add_u32 s64, s64, 32768\n"
    "v_bfe_u32 v10, v1, 2, 2\n"
    "v_lshrrev_b32 v11, 5, v1\n"
    "v_lshl_add_u32 v10, v11, 2, v10\n"
    "v_lshlrev_b32 v10, 6, v10\n"
    "v_bfe_u32 v12, v1, 4, 1\n"
    "v_lshl_or_b32 v10, v12, 5, v10\n"
    "v_and_b32 v13, 3, v1\n"
    "v_lshl_or_b32 v10, v13, 3, v10\n"
    "v_add_u32 v6, s64, v10\n"
    "v_and_b32 v10, 31, v1\n"
    "v_lshlrev_b32 v8, 3, v10\n"
    "v_lshlrev_b32 v11, 4, v10\n"
    "s_lshr_b32 s55, s28, 1\n"
    "s_lshl_b32 s55, s55, 9\n"
    "s_and_b32 s56, s28, 1\n"
    "s_lshl3_add_u32 s55, s56, s55\n"
    "v_add_u32 v12, s55, v11\n"
    "s_mov_b32 s59, 0\n"
    "s_mov_b32 s58, 0x3f803f80\n"
    "s_lshl2_add_u32 s40, s26, s27\n"
    "s_sub_u32 s40, s40, 28\n"
    "s_setprio 0\n"
    "v_bfe_u32 v18, v1, 4, 2\n"
    "v_lshlrev_b32 v19, 1, v18\n"
    "v_and_b32 v20, 7, v1\n"
    "v_xor_b32 v19, v19, v20\n"
    "v_lshrrev_b32 v20, 3, v1\n"
    "v_mul_u32_u24 v20, 896, v20\n"
    "v_lshl_add_u32 v24, v19, 4, v20\n"
    "v_add_u32 v25, 7168, v24\n"
    "v_add_u32 v26, 14336, v24\n"
    "v_add_u32 v27, 21504, v24\n"
    "v_bfe_u32 v19, v1, 2, 2\n"
    "v_and_b32 v20, 1, v18\n"
    "v_lshl_or_b32 v19, v20, 2, v19\n"
    "v_lshrrev_b32 v20, 1, v18\n"
    "v_lshl_or_b32 v19, v20, 4, v19\n"
    "v_bfe_u32 v20, v19, 1, 2\n"
    "v_lshlrev_b32 v20, 5, v20\n"
    "v_lshl_or_b32 v20, v19, 7, v20\n"
    "v_and_b32 v19, 3, v1\n"
    "v_lshl_or_b32 v28, v19, 3, v20\n"
    "s_lshl3_add_u32 s36, s40, s28\n"
    "s_lshl_b32 s42, s28, 13\n"
    "s_add_u32 s42, s42, 65536\n"
    "s_mov_b32 s45, 4096\n"
    ".Lk_w183:\n"
    "s_memrealtime s[60:61]\n"
    "s_waitcnt lgkmcnt(0)\n"
    "s_sub_u32 s45, s45, 1\n"
    "s_cbranch_scc1 .Lk_x184\n"
    "s_sub_u32 s60, s60, s54\n"
    "s_cmp_lt_u32 s60, 100\n"
    "s_cbranch_scc1 .Lk_w183\n"
    ".Lk_x184:\n"
    "s_add_u32 s57, s36, 0\n"
    "s_mul_i32 s37, s57, 9363\n"
    "s_lshr_b32 s37, s37, 16\n"
    "s_mul_i32 s55, s37, 7\n"
    "s_sub_u32 s39, s57, s55\n"
    "s_mul_i32 s55, s37, 28672\n"
    "s_lshl_b32 s56, s39, 7\n"
    "s_add_u32 s55, s55, s56\n"
    "s_add_u32 s46, s20, s55\n"
    "s_addc_u32 s47, s21, 0\n"
    "s_add_u32 m0, s42, 0\n"
    "s_nop 0\n"
    "global_load_lds_dwordx4 v24, s[46:47] offset:0 nt\n"
    "s_add_u32 m0, s42, 1024\n"
    "s_nop 0\n"
    "global_load_lds_dwordx4 v25, s[46:47] offset:0 nt\n"
    "s_add_u32 m0, s42, 2048\n"
    "s_nop 0\n"
    "global_load_lds_dwordx4 v26, s[46:47] offset:0 nt\n"
    "s_add_u32 m0, s42, 3072\n"
    "s_nop 0\n"
    "global_load_lds_dwordx4 v27, s[46:47] offset:0 nt\n"
    "s_cmp_ge_u32 s36, 136\n"
    "s_cbranch_scc1 .Lk_s185\n"
    "s_add_u32 s57, s36, 256\n"
    "s_mul_i32 s38, s57, 9363\n"
    "s_lshr_b32 s38, s38, 16\n"
    "s_mul_i32 s55, s38, 7\n"
    "s_sub_u32 s43, s57, s55\n"
    "s_mul_i32 s55, s38, 28672\n"
    "s_lshl_b32 s56, s43, 7\n"
    "s_add_u32 s55, s55, s56\n"
    "s_add_u32 s46, s20, s55\n"
    "s_addc_u32 s47, s21, 0\n"
    "s_add_u32 m0, s42, 4096\n"
    "s_nop 0\n"
    "global_load_lds_dwordx4 v24, s[46:47] offset:0 nt\n"
    "s_add_u32 m0, s42, 5120\n"
    "s_nop 0\n"
    "global_load_lds_dwordx4 v25, s[46:47] offset:0 nt\n"
    "s_add_u32 m0, s42, 6144\n"
    "s_nop 0\n"
    "global_load_lds_dwordx4 v26, s[46:47] offset:0 nt\n"
    "s_add_u32 m0, s42, 7168\n"
    "s_nop 0\n"
    "global_load_lds_dwordx4 v27, s[46:47] offset:0 nt\n"
    ".Lk_s185:\n"
    "s_waitcnt vmcnt(0)\n"
    "s_mul_i32 s57, s37, 9363\n"
    "s_lshr_b32 s57, s57, 16\n"
    "s_and_b32 s55, s57, 3\n"
    "s_lshl_b32 s55, s55, 5\n"
    "v_xor_b32 v29, s55, v28\n"
    "v_add_u32 v29, s42, v29\n"
    "v_xor_b32 v30, 32, v29\n"
    "v_xor_b32 v31, 64, v29\n"
    "v_xor_b32 v32, 96, v29\n"
    "ds_read_b64_tr_b16 v[48:49], v29 offset:0\n"
    "ds_read_b64_tr_b16 v[50:51], v29 offset:1024\n"
    "ds_read_b64_tr_b16 v[52:53], v30 offset:0\n"
    "ds_read_b64_tr_b16 v[54:55], v30 offset:1024\n"
    "ds_read_b64_tr_b16 v[56:57], v31 offset:0\n"
    "ds_read_b64_tr_b16 v[58:59], v31 offset:1024\n"
    "ds_read_b64_tr_b16 v[60:61], v32 offset:0\n"
    "ds_read_b64_tr_b16 v[62:63], v32 offset:1024\n"
    "s_mul_i32 s55, s39, 56\n"
    "s_add_u32 s55, s55, s37\n"
    "s_lshl_b32 s55, s55, 12\n"
    "s_add_u32 s55, s55, 16498688\n"
    "s_add_u32 s46, s22, s55\n"
    "s_addc_u32 s47, s23, 0\n"
    "s_lshl_b32 s55, s39, 6\n"
    "s_add_u32 s55, s55, s37\n"
    "s_add_u32 s55, s55, s57\n"
    "s_lshl_b32 s55, s55, 4\n"
    "s_add_u32 s55, s55, 18118656\n"
    "s_add_u32 s48, s22, s55\n"
    "s_addc_u32 s49, s23, 0\n"
    "s_waitcnt lgkmcnt(6)\n"
    "global_store_dwordx4 v2, v[48:51], s[46:47] offset:0 sc1\n"
    "s_waitcnt lgkmcnt(4)\n"
    "global_store_dwordx4 v2, v[52:55], s[46:47] offset:1024 sc1\n"
    "s_waitcnt lgkmcnt(2)\n"
    "global_store_dwordx4 v2, v[56:59], s[46:47] offset:2048 sc1\n"
    "s_waitcnt lgkmcnt(0)\n"
    "global_store_dwordx4 v2, v[60:63], s[46:47] offset:3072 sc1\n"
    "s_cmp_ge_u32 s36, 136\n"
    "s_cbranch_scc1 .Lk_s186\n"
    "s_mul_i32 s57, s38, 9363\n"
    "s_lshr_b32 s57, s57, 16\n"
    "s_and_b32 s55, s57, 3\n"
    "s_lshl_b32 s55, s55, 5\n"
    "v_xor_b32 v29, s55, v28\n"
    "v_add_u32 v29, s42, v29\n"
    "v_xor_b32 v30, 32, v29\n"
    "v_xor_b32 v31, 64, v29\n"
    "v_xor_b32 v32, 96, v29\n"
    "ds_read_b64_tr_b16 v[64:65], v29 offset:4096\n"
    "ds_read_b64_tr_b16 v[66:67], v29 offset:5120\n"
    "ds_read_b64_tr_b16 v[68:69], v30 offset:4096\n"
    "ds_read_b64_tr_b16 v[70:71], v30 offset:5120\n"
    "ds_read_b64_tr_b16 v[72:73], v31 offset:4096\n"
    "ds_read_b64_tr_b16 v[74:75], v31 offset:5120\n"
    "ds_read_b64_tr_b16 v[76:77], v32 offset:4096\n"
    "ds_read_b64_tr_b16 v[78:79], v32 offset:5120\n"
    "s_mul_i32 s55, s43, 56\n"
    "s_add_u32 s55, s55, s38\n"
    "s_lshl_b32 s55, s55, 12\n"
    "s_add_u32 s55, s55, 16498688\n"
    "s_add_u32 s46, s22, s55\n"
    "s_addc_u32 s47, s23, 0\n"
    "s_lshl_b32 s55, s43, 6\n"
    "s_add_u32 s55, s55, s38\n"
    "s_add_u32 s55, s55, s57\n"
    "s_lshl_b32 s55, s55, 4\n"
    "s_add_u32 s55, s55, 18118656\n"
    "s_add_u32 s50, s22, s55\n"
    "s_addc_u32 s51, s23, 0\n"
    "s_waitcnt lgkmcnt(6)\n"
    "global_store_dwordx4 v2, v[64:67], s[46:47] offset:0 sc1\n"
    "s_waitcnt lgkmcnt(4)\n"
    "global_store_dwordx4 v2, v[68:71], s[46:47] offset:1024 sc1\n"
    "s_waitcnt lgkmcnt(2)\n"
    "global_store_dwordx4 v2, v[72:75], s[46:47] offset:2048 sc1\n"
    "s_waitcnt lgkmcnt(0)\n"
    "global_store_dwordx4 v2, v[76:79], s[46:47] offset:3072 sc1\n"
    ".Lk_s186:\n"
    "s_waitcnt vmcnt(0)\n"
    "s_mov_b64 exec, 1\n"
    "global_store_dwordx2 v3, v[4:5], s[48:49] sc1\n"
    "s_mov_b64 exec, -1\n"
    "s_cmp_ge_u32 s36, 136\n"
    "s_cbranch_scc1 .Lk_s187\n"
    "s_mov_b64 exec, 1\n"
    "global_store_dwordx2 v3, v[4:5], s[50:51] sc1\n"
    "s_mov_b64 exec, -1\n"
    ".Lk_s187:\n"
    "s_cmp_ge_u32 s40, 28\n"
    "s_cselect_b32 s55, 4, s28\n"
    "s_cmp_ge_u32 s55, 4\n"
    "s_cbranch_scc1 .Lk_s188\n"
    "s_lshl2_add_u32 s36, s40, s28\n"
    "s_lshr_b32 s55, s36, 2\n"
    "s_lshl_b32 s55, s55, 9\n"
    "s_add_u32 s55, s55, 18104320\n"
    "s_add_u32 s48, s22, s55\n"
    "s_addc_u32 s49, s23, 0\n"
    "s_mul_i32 s55, s36, 28672\n"
    "s_add_u32 s55, s55, 13287424\n"
    "s_add_u32 s8, s22, s55\n"
    "s_addc_u32 s9, s23, 0\n"
    "v_min_u32 v18, 27, v1\n"
    "v_lshlrev_b32 v18, 4, v18\n"
    "v_mov_b32 v20, 0x3f803f80\n"
    "v_mov_b32 v21, 0x3f803f80\n"
    "s_mov_b32 s45, 4096\n"
    ".Lk_w189:\n"
    "s_memrealtime s[60:61]\n"
    "s_waitcnt lgkmcnt(0)\n"
    "s_sub_u32 s45, s45, 1\n"
    "s_cbranch_scc1 .Lk_x190\n"
    "s_sub_u32 s60, s60, s54\n"
    "s_cmp_lt_u32 s60, 390\n"
    "s_cbranch_scc1 .Lk_w189\n"
    ".Lk_x190:\n"
    "global_load_dwordx2 v[40:41], v18, s[48:49] sc1\n"
    "s_waitcnt vmcnt(0)\n"
    "v_cmp_ne_u64 vcc, s[34:35], v[40:41]\n"
    "s_cbranch_vccnz .Lk_r191\n"
    ".Lk_c192:\n"
    "s_add_u32 s94, s8, 0\n"
    "s_addc_u32 s95, s9, 0\n"
    "global_load_dwordx4 v[144:147], v2, s[94:95] offset:0 sc1\n"
    "global_load_dwordx4 v[148:151], v2, s[94:95] offset:1024 sc1\n"
    "global_load_dwordx4 v[152:155], v2, s[94:95] offset:2048 sc1\n"
    "global_load_dwordx4 v[156:159], v2, s[94:95] offset:3072 sc1\n"
    "s_add_u32 s94, s8, 4096\n"
    "s_addc_u32 s95, s9, 0\n"
    "global_load_dwordx4 v[160:163], v2, s[94:95] offset:0 sc1\n"
    "global_load_dwordx4 v[164:167], v2, s[94:95] offset:1024 sc1\n"
    "global_load_dwordx4 v[168:171], v2, s[94:95] offset:2048 sc1\n"
    "global_load_dwordx4 v[172:175], v2, s[94:95] offset:3072 sc1\n"
    "s_add_u32 s94, s8, 8192\n"
    "s_addc_u32 s95, s9, 0\n"
    "global_load_dwordx4 v[176:179], v2, s[94:95] offset:0 sc1\n"
    "global_load_dwordx4 v[180:183], v2, s[94:95] offset:1024 sc1\n"
    "global_load_dwordx4 v[184:187], v2, s[94:95] offset:2048 sc1\n"
    "global_load_dwordx4 v[188:191], v2, s[94:95] offset:3072 sc1\n"
    "s_add_u32 s94, s8, 12288\n"
    "s_addc_u32 s95, s9, 0\n"
    "global_load_dwordx4 v[192:195], v2, s[94:95] offset:0 sc1\n"
    "global_load_dwordx4 v[196:199], v2, s[94:95] offset:1024 sc1\n"
    "global_load_dwordx4 v[200:203], v2, s[94:95] offset:2048 sc1\n"
    "global_load_dwordx4 v[204:207], v2, s[94:95] offset:3072 sc1\n"
    "s_add_u32 s94, s8, 16384\n"
    "s_addc_u32 s95, s9, 0\n"
    "global_load_dwordx4 v[208:211], v2, s[94:95] offset:0 sc1\n"
    "global_load_dwordx4 v[212:215], v2, s[94:95] offset:1024 sc1\n"
    "global_load_dwordx4 v[216:219], v2, s[94:95] offset:2048 sc1\n"
    "global_load_dwordx4 v[220:223], v2, s[94:95] offset:3072 sc1\n"
    "s_add_u32 s94, s8, 20480\n"
    "s_addc_u32 s95, s9, 0\n"
    "global_load_dwordx4 v[224:227], v2, s[94:95] offset:0 sc1\n"
    "global_load_dwordx4 v[228:231], v2, s[94:95] offset:1024 sc1\n"
    "global_load_dwordx4 v[232:235], v2, s[94:95] offset:2048 sc1\n"
    "global_load_dwordx4 v[236:239], v2, s[94:95] offset:3072 sc1\n"
    "s_add_u32 s94, s8, 24576\n"
    "s_addc_u32 s95, s9, 0\n"
    "global_load_dwordx4 v[240:243], v2, s[94:95] offset:0 sc1\n"
    "global_load_dwordx4 v[244:247], v2, s[94:95] offset:1024 sc1\n"
    "global_load_dwordx4 v[248:251], v2, s[94:95] offset:2048 sc1\n"
    "global_load_dwordx4 v[252:255], v2, s[94:95] offset:3072 sc1\n"
    "s_lshl_b32 s55, s36, 8\n"
    "s_add_u32 s55, s55, 18126848\n"
    "s_add_u32 s46, s22, s55\n"
    "s_addc_u32 s47, s23, 0\n"
    "s_waitcnt vmcnt(27)\n"
    "v_mfma_f32_4x4x4_16b_bf16 v[24:27], v[20:21], v[144:145], 0\n"
    "v_mfma_f32_4x4x4_16b_bf16 v[28:31], v[20:21], v[146:147], 0\n"
    "s_waitcnt vmcnt(26)\n"
    "v_mfma_f32_4x4x4_16b_bf16 v[32:35], v[20:21], v[148:149], 0\n"
    "v_mfma_f32_4x4x4_16b_bf16 v[24:27], v[20:21], v[150:151], v[24:27]\n"
    "s_waitcnt vmcnt(25)\n"
    "v_mfma_f32_4x4x4_16b_bf16 v[28:31], v[20:21], v[152:153], v[28:31]\n"
    "v_mfma_f32_4x4x4_16b_bf16 v[32:35], v[20:21], v[154:155], v[32:35]\n"
    "s_waitcnt vmcnt(24)\n"
    "v_mfma_f32_4x4x4_16b_bf16 v[24:27], v[20:21], v[156:157], v[24:27]\n"
    "v_mfma_f32_4x4x4_16b_bf16 v[28:31], v[20:21], v[158:159], v[28:31]\n"
    "s_waitcnt vmcnt(23)\n"
    "v_mfma_f32_4x4x4_16b_bf16 v[32:35], v[20:21], v[160:161], v[32:35]\n"
    "v_mfma_f32_4x4x4_16b_bf16 v[24:27], v[20:21], v[162:163], v[24:27]\n"
    "s_waitcnt vmcnt(22)\n"
    "v_mfma_f32_4x4x4_16b_bf16 v[28:31], v[20:21], v[164:165], v[28:31]\n"
    "v_mfma_f32_4x4x4_16b_bf16 v[32:35], v[20:21], v[166:167], v[32:35]\n"
    "s_waitcnt vmcnt(21)\n"
    "v_mfma_f32_4x4x4_16b_bf16 v[24:27], v[20:21], v[168:169], v[24:27]\n"
    "v_mfma_f32_4x4x4_16b_bf16 v[28:31], v[20:21], v[170:171], v[28:31]\n"
    "s_waitcnt vmcnt(20)\n"
    "v_mfma_f32_4x4x4_16b_bf16 v[32:35], v[20:21], v[172:173], v[32:35]\n"
    "v_mfma_f32_4x4x4_16b_bf16 v[24:27], v[20:21], v[174:175], v[24:27]\n"
    "s_waitcnt vmcnt(19)\n"
    "v_mfma_f32_4x4x4_16b_bf16 v[28:31], v[20:21], v[176:177], v[28:31]\n"
    "v_mfma_f32_4x4x4_16b_bf16 v[32:35], v[20:21], v[178:179], v[32:35]\n"
    "s_waitcnt vmcnt(18)\n"
    "v_mfma_f32_4x4x4_16b_bf16 v[24:27], v[20:21], v[180:181], v[24:27]\n"
    "v_mfma_f32_4x4x4_16b_bf16 v[28:31], v[20:21], v[182:183], v[28:31]\n"
    "s_waitcnt vmcnt(17)\n"
    "v_mfma_f32_4x4x4_16b_bf16 v[32:35], v[20:21], v[184:185], v[32:35]\n"
    "v_mfma_f32_4x4x4_16b_bf16 v[24:27], v[20:21], v[186:187], v[24:27]\n"
    "s_waitcnt vmcnt(16)\n"
    "v_mfma_f32_4x4x4_16b_bf16 v[28:31], v[20:21], v[188:189], v[28:31]\n"
    "v_mfma_f32_4x4x4_16b_bf16 v[32:35], v[20:21], v[190:191], v[32:35]\n"
    "s_waitcnt vmcnt(15)\n"
    "v_mfma_f32_4x4x4_16b_bf16 v[24:27], v[20:21], v[192:193], v[24:27]\n"
    "v_mfma_f32_4x4x4_16b_bf16 v[28:31], v[20:21], v[194:195], v[28:31]\n"
    "s_waitcnt vmcnt(14)\n"
    "v_mfma_f32_4x4x4_16b_bf16 v[32:35], v[20:21], v[196:197], v[32:35]\n"
    "v_mfma_f32_4x4x4_16b_bf16 v[24:27], v[20:21], v[198:199], v[24:27]\n"
    "s_waitcnt vmcnt(13)\n"
    "v_mfma_f32_4x4x4_16b_bf16 v[28:31], v[20:21], v[200:201], v[28:31]\n"
    "v_mfma_f32_4x4x4_16b_bf16 v[32:35], v[20:21], v[202:203], v[32:35]\n"
    "s_waitcnt vmcnt(12)\n"
    "v_mfma_f32_4x4x4_16b_bf16 v[24:27], v[20:21], v[204:205], v[24:27]\n"
    "v_mfma_f32_4x4x4_16b_bf16 v[28:31], v[20:21], v[206:207], v[28:31]\n"
    "s_waitcnt vmcnt(11)\n"
    "v_mfma_f32_4x4x4_16b_bf16 v[32:35], v[20:21], v[208:209], v[32:35]\n"
    "v_mfma_f32_4x4x4_16b_bf16 v[24:27], v[20:21], v[210:211], v[24:27]\n"
    "s_waitcnt vmcnt(10)\n"
    "v_mfma_f32_4x4x4_16b_bf16 v[28:31], v[20:21], v[212:213], v[28:31]\n"
    "v_mfma_f32_4x4x4_16b_bf16 v[32:35], v[20:21], v[214:215], v[32:35]\n"
    "s_waitcnt vmcnt(9)\n"
    "v_mfma_f32_4x4x4_16b_bf16 v[24:27], v[20:21], v[216:217], v[24:27]\n"
    "v_mfma_f32_4x4x4_16b_bf16 v[28:31], v[20:21], v[218:219], v[28:31]\n"
    "s_waitcnt vmcnt(8)\n"
    "v_mfma_f32_4x4x4_16b_bf16 v[32:35], v[20:21], v[220:221], v[32:35]\n"
    "v_mfma_f32_4x4x4_16b_bf16 v[24:27], v[20:21], v[222:223], v[24:27]\n"
    "s_waitcnt vmcnt(7)\n"
    "v_mfma_f32_4x4x4_16b_bf16 v[28:31], v[20:21], v[224:225], v[28:31]\n"
    "v_mfma_f32_4x4x4_16b_bf16 v[32:35], v[20:21], v[226:227], v[32:35]\n"
    "s_waitcnt vmcnt(6)\n"
    "v_mfma_f32_4x4x4_16b_bf16 v[24:27], v[20:21], v[228:229], v[24:27]\n"
    "v_mfma_f32_4x4x4_16b_bf16 v[28:31], v[20:21], v[230:231], v[28:31]\n"
    "s_waitcnt vmcnt(5)\n"
    "v_mfma_f32_4x4x4_16b_bf16 v[32:35], v[20:21], v[232:233], v[32:35]\n"
    "v_mfma_f32_4x4x4_16b_bf16 v[24:27], v[20:21], v[234:235], v[24:27]\n"
    "s_waitcnt vmcnt(4)\n"
    "v_mfma_f32_4x4x4_16b_bf16 v[28:31], v[20:21], v[236:237], v[28:31]\n"
    "v_mfma_f32_4x4x4_16b_bf16 v[32:35], v[20:21], v[238:239], v[32:35]\n"
    "s_waitcnt vmcnt(3)\n"
    "v_mfma_f32_4x4x4_16b_bf16 v[24:27], v[20:21], v[240:241], v[24:27]\n"
    "v_mfma_f32_4x4x4_16b_bf16 v[28:31], v[20:21], v[242:243], v[28:31]\n"
    "s_waitcnt vmcnt(2)\n"
    "v_mfma_f32_4x4x4_16b_bf16 v[32:35], v[20:21], v[244:245], v[32:35]\n"
    "v_mfma_f32_4x4x4_16b_bf16 v[24:27], v[20:21], v[246:247], v[24:27]\n"
    "s_waitcnt vmcnt(1)\n"
    "v_mfma_f32_4x4x4_16b_bf16 v[28:31], v[20:21], v[248:249], v[28:31]\n"
    "v_mfma_f32_4x4x4_16b_bf16 v[32:35], v[20:21], v[250:251], v[32:35]\n"
    "s_waitcnt vmcnt(0)\n"
    "v_mfma_f32_4x4x4_16b_bf16 v[24:27], v[20:21], v[252:253], v[24:27]\n"
    "v_mfma_f32_4x4x4_16b_bf16 v[28:31], v[20:21], v[254:255], v[28:31]\n"
    "s_nop 4\n"
    "v_add_f32 v24, v24, v28\n"
    "v_add_f32 v24, v24, v32\n"
    "v_mov_b32 v25, v24\n"
    "s_nop 1\n"
    "v_permlane32_swap_b32 v24, v25\n"
    "v_add_f32 v24, v24, v25\n"
    "v_cvt_pk_bf16_f32 v25, v24, v24\n"
    "v_and_b32 v25, 0xffff0000, v25\n"
    "v_sub_f32 v26, v24, v25\n"
    "v_cvt_pk_bf16_f32 v24, v25, v26\n"
    "v_mov_b32 v25, s34\n"
    "s_mov_b64 exec, 0xffffffff\n"
    "global_store_dwordx2 v8, v[24:25], s[46:47] sc1\n"
    "s_mov_b64 exec, -1\n"
    ".Lk_s188:\n"
    "s_lshl_b32 s55, s40, 7\n"
    "s_add_u32 s55, s55, 18155520\n"
    "s_add_u32 s52, s22, s55\n"
    "s_addc_u32 s53, s23, 0\n"
    "global_load_dwordx2 v[40:41], v3, s[52:53] sc1\n"
    "s_waitcnt vmcnt(0)\n"
    "v_cmp_ne_u64 vcc, s[34:35], v[40:41]\n"
    "s_cbranch_vccnz .Lk_r194\n"
    ".Lk_c195:\n"
    "s_sleep 14\n"
    "global_load_dwordx2 v[40:41], v7, s[78:79] sc1\n"
    "s_waitcnt vmcnt(0)\n"
    ".Lk_hl:\n"
    "v_cmp_ne_u64 vcc, s[34:35], v[40:41]\n"
    "s_cbranch_vccnz .Lk_r197\n"
    ".Lk_c198:\n"
    "s_mov_b32 m0, s64\n"
    "s_nop 0\n"
    "global_load_lds_dwordx4 v2, s[80:81] offset:0 sc1\n"
    "global_load_lds_dwordx4 v2, s[80:81] offset:1024 sc1\n"
    "global_load_lds_dwordx4 v2, s[80:81] offset:2048 sc1\n"
    "s_mov_b64 exec, 0xffffffff\n"
    "global_load_lds_dwordx4 v2, s[80:81] offset:3072 sc1\n"
    "s_mov_b64 exec, -1\n"
    "v_mov_b32 v14, 0\n"
    "v_mov_b32 v15, 0\n"
    "v_mov_b32 v16, 0\n"
    "v_mov_b32 v17, 0\n"
    "s_cmp_lt_u32 s59, 3\n"
    "s_cselect_b32 s55, 32768, 0\n"
    "s_add_u32 s78, s78, s55\n"
    "s_addc_u32 s79, s79, 0\n"
    "s_waitcnt vmcnt(0)\n"
    "s_cmp_eq_u32 s59, 1\n"
    "s_cbranch_scc1 .Lk_s200\n"
    "global_load_dwordx2 v[40:41], v7, s[78:79] sc1\n"
    ".Lk_s200:\n"
    "ds_read_b64_tr_b16 v[48:49], v6 offset:0\n"
    "ds_read_b64_tr_b16 v[50:51], v6 offset:512\n"
    "ds_read_b64_tr_b16 v[52:53], v6 offset:1024\n"
    "ds_read_b64_tr_b16 v[54:55], v6 offset:1536\n"
    "ds_read_b64_tr_b16 v[56:57], v6 offset:2048\n"
    "ds_read_b64_tr_b16 v[58:59], v6 offset:2560\n"
    "ds_read_b64_tr_b16 v[60:61], v6 offset:3072\n"
    "s_waitcnt lgkmcnt(6)\n"
    "v_dot2c_f32_bf16 v14, s58, v48\n"
    "v_dot2c_f32_bf16 v15, v48, v48\n"
    "v_dot2c_f32_bf16 v16, s58, v49\n"
    "v_dot2c_f32_bf16 v17, v49, v49\n"
    "s_waitcnt lgkmcnt(5)\n"
    "v_dot2c_f32_bf16 v14, s58, v50\n"
    "v_dot2c_f32_bf16 v15, v50, v50\n"
    "v_dot2c_f32_bf16 v16, s58, v51\n"
    "v_dot2c_f32_bf16 v17, v51, v51\n"
    "s_waitcnt lgkmcnt(4)\n"
    "v_dot2c_f32_bf16 v14, s58, v52\n"
    "v_dot2c_f32_bf16 v15, v52, v52\n"
    "v_dot2c_f32_bf16 v16, s58, v53\n"
    "v_dot2c_f32_bf16 v17, v53, v53\n"
    "s_waitcnt lgkmcnt(3)\n"
    "v_dot2c_f32_bf16 v14, s58, v54\n"
    "v_dot2c_f32_bf16 v15, v54, v54\n"
    "v_dot2c_f32_bf16 v16, s58, v55\n"
    "v_dot2c_f32_bf16 v17, v55, v55\n"
    "s_waitcnt lgkmcnt(2)\n"
    "v_dot2c_f32_bf16 v14, s58, v56\n"
    "v_dot2c_f32_bf16 v15, v56, v56\n"
    "v_dot2c_f32_bf16 v16, s58, v57\n"
    "v_dot2c_f32_bf16 v17, v57, v57\n"
    "s_waitcnt lgkmcnt(1)\n"
    "v_dot2c_f32_bf16 v14, s58, v58\n"
    "v_dot2c_f32_bf16 v15, v58, v58\n"
    "v_dot2c_f32_bf16 v16, s58, v59\n"
    "v_dot2c_f32_bf16 v17, v59, v59\n"
    "s_waitcnt lgkmcnt(0)\n"
    "v_dot2c_f32_bf16 v14, s58, v60\n"
    "v_dot2c_f32_bf16 v15, v60, v60\n"
    "v_dot2c_f32_bf16 v16, s58, v61\n"
    "v_dot2c_f32_bf16 v17, v61, v61\n"
    "s_nop 2\n"
    "v_pk_add_f32 v[14:15], v[14:15], v[16:17]\n"
    "v_mov_b32 v16, v14\n"
    "v_mov_b32 v17, v15\n"
    "s_nop 0\n"
    "v_permlane32_swap_b32 v14, v16\n"
    "v_permlane32_swap_b32 v15, v17\n"
    "v_pk_add_f32 v[14:15], v[14:15], v[16:17]\n"
    "ds_write_b64 v12, v[14:15]\n"
    "s_waitcnt lgkmcnt(0)\n"
    "s_barrier\n"
    "s_cmp_lg_u32 s28, 0\n"
    "s_cbranch_scc1 .Lk_hn\n"
    "ds_read_b128 v[48:51], v11 offset:0\n"
    "ds_read_b128 v[52:55], v11 offset:512\n"
    "ds_read_b128 v[56:59], v11 offset:1024\n"
    "ds_read_b128 v[60:63], v11 offset:1536\n"
    "s_waitcnt lgkmcnt(3)\n"
    "v_pk_add_f32 v[48:49], v[48:49], v[50:51]\n"
    "s_waitcnt lgkmcnt(2)\n"
    "v_pk_add_f32 v[48:49], v[48:49], v[52:53]\n"
    "v_pk_add_f32 v[48:49], v[48:49], v[54:55]\n"
    "s_waitcnt lgkmcnt(1)\n"
    "v_pk_add_f32 v[48:49], v[48:49], v[56:57]\n"
    "v_pk_add_f32 v[48:49], v[48:49], v[58:59]\n"
    "s_waitcnt lgkmcnt(0)\n"
    "v_pk_add_f32 v[48:49], v[48:49], v[60:61]\n"
    "v_pk_add_f32 v[48:49], v[48:49], v[62:63]\n"
    "v_mul_f32 v48, 0x3b124925, v48\n"
    "v_mul_f32 v49, 0x3b124925, v49\n"
    "v_fma_f32 v49, -v48, v48, v49\n"
    "v_max_f32 v49, 0, v49\n"
    "v_cvt_pk_bf16_f32 v49, v49, v49\n"
    "v_and_b32 v49, 0xffff0000, v49\n"
    "v_add_f32 v49, 0x3727c5ac, v49\n"
    "v_cvt_pk_bf16_f32 v49, v49, v49\n"
    "v_and_b32 v49, 0xffff0000, v49\n"
    "v_sqrt_f32 v49, v49\n"
    "s_nop 0\n"
    "v_cvt_pk_bf16_f32 v50, v48, v49\n"
    "v_mov_b32 v51, s34\n"
    "s_mov_b64 exec, 0xffffffff\n"
    "global_store_dwordx2 v8, v[50:51], s[82:83] sc1\n"
    "s_mov_b64 exec, -1\n"
    "s_waitcnt vmcnt(1)\n"
    "s_branch .Lk_hm\n"
    ".Lk_hn:\n"
    "s_waitcnt vmcnt(0)\n"
    ".Lk_hm:\n"
    "s_cmp_lg_u32 s59, 1\n"
    "s_cbranch_scc1 .Lk_s201\n"
    "s_add_u32 s52, s52, 4096\n"
    "s_addc_u32 s53, s53, 0\n"
    "global_load_dwordx2 v[40:41], v3, s[52:53] sc1\n"
    "s_waitcnt vmcnt(0)\n"
    "v_cmp_ne_u64 vcc, s[34:35], v[40:41]\n"
    "s_cbranch_vccnz .Lk_r202\n"
    ".Lk_c203:\n"
    "s_sleep 14\n"
    "global_load_dwordx2 v[40:41], v7, s[78:79] sc1\n"
    "s_waitcnt vmcnt(0)\n"
    ".Lk_s201:\n"
    "s_add_u32 s80, s80, 458752\n"
    "s_addc_u32 s81, s81, 0\n"
    "s_add_u32 s82, s82, 4096\n"
    "s_addc_u32 s83, s83, 0\n"
    "v_xor_b32 v11, 2048, v11\n"
    "v_xor_b32 v12, 2048, v12\n"
    "s_add_u32 s59, s59, 1\n"
    "s_cmp_lt_u32 s59, 4\n"
    "s_cbranch_scc1 .Lk_hl\n"
    "s_endpgm\n"
    ".Lk_t0:\n"
    "v_mov_b32 v32, 0\n"
    "v_mov_b32 v33, 0\n"
    "v_mov_b32 v34, 0\n"
    "s_branch .Lk_t0b\n"
    ".Lk_r3:\n"
    "s_mov_b32 s45, 131072\n"
    ".Lk_l5:\n"
    "s_sleep 3\n"
    "global_load_dwordx2 v[32:33], v6, s[46:47] sc1\n"
    "s_waitcnt vmcnt(0)\n"
    "v_cmp_ne_u64 vcc, s[34:35], v[32:33]\n"
    "s_cbranch_vccz .Lk_c4\n"
    "s_sub_u32 s45, s45, 1\n"
    "s_cbranch_scc0 .Lk_l5\n"
    "s_branch .Lk_c4\n"
    ".Lk_r6:\n"
    "s_mov_b32 s45, 131072\n"
    ".Lk_l8:\n"
    "s_sleep 1\n"
    "global_load_dwordx4 v[240:243], v18, s[50:51] offset:0 sc1\n"
    "global_load_dwordx4 v[244:247], v18, s[50:51] offset:16 sc1\n"
    "s_waitcnt vmcnt(0)\n"
    "v_bitop3_b32 v241, v241, v243, s34 bitop3:0x7e\n"
    "v_bitop3_b32 v245, v245, v247, s34 bitop3:0x7e\n"
    "v_or_b32 v241, v241, v245\n"
    "v_cmp_ne_u32 vcc, 0, v241\n"
    "s_cbranch_vccz .Lk_c7\n"
    "s_sub_u32 s45, s45, 1\n"
    "s_cbranch_scc0 .Lk_l8\n"
    "s_branch .Lk_c7\n"
    ".Lk_r9:\n"
    "s_mov_b32 s45, 131072\n"
    ".Lk_l11:\n"
    "s_sleep 3\n"
    "global_load_dwordx2 v[20:21], v8, s[66:67] sc1\n"
    "s_waitcnt vmcnt(0)\n"
    "v_cmp_ne_u64 vcc, s[34:35], v[20:21]\n"
    "s_cbranch_vccz .Lk_c10\n"
    "s_sub_u32 s45, s45, 1\n"
    "s_cbranch_scc0 .Lk_l11\n"
    "s_branch .Lk_c10\n"
    ".Lk_r32:\n"
    "s_mov_b32 s45, 131072\n"
    ".Lk_l34:\n"
    "s_sleep 1\n"
    "global_load_dwordx4 v[240:243], v18, s[50:51] offset:0 sc1\n"
    "global_load_dwordx4 v[244:247], v18, s[50:51] offset:16 sc1\n"
    "s_waitcnt vmcnt(0)\n"
    "v_bitop3_b32 v241, v241, v243, s34 bitop3:0x7e\n"
    "v_bitop3_b32 v245, v245, v247, s34 bitop3:0x7e\n"
    "v_or_b32 v241, v241, v245\n"
    "v_cmp_ne_u32 vcc, 0, v241\n"
    "s_cbranch_vccz .Lk_c33\n"
    "s_sub_u32 s45, s45, 1\n"
    "s_cbranch_scc0 .Lk_l34\n"
    "s_branch .Lk_c33\n"
    ".Lk_r57:\n"
    "s_mov_b32 s45, 131072\n"
    ".Lk_l59:\n"
    "s_sleep 3\n"
    "global_load_dwordx2 v[20:21], v15, s[78:79] sc1\n"
    "s_waitcnt vmcnt(0)\n"
    "v_cmp_ne_u64 vcc, s[34:35], v[20:21]\n"
    "s_cbranch_vccz .Lk_c58\n"
    "s_sub_u32 s45, s45, 1\n"
    "s_cbranch_scc0 .Lk_l59\n"
    "s_branch .Lk_c58\n"
    ".Lk_r60:\n"
    "s_mov_b32 s45, 131072\n"
    ".Lk_l62:\n"
    "s_sleep 3\n"
    "global_load_dwordx2 v[24:25], v9, s[60:61] sc1\n"
    "s_waitcnt vmcnt(0)\n"
    "v_cmp_ne_u64 vcc, s[34:35], v[24:25]\n"
    "s_cbranch_vccz .Lk_c61\n"
    "s_sub_u32 s45, s45, 1\n"
    "s_cbranch_scc0 .Lk_l62\n"
    "s_branch .Lk_c61\n"
    ".Lk_r63:\n"
    "s_mov_b32 s45, 131072\n"
    ".Lk_l65:\n"
    "s_sleep 3\n"
    "global_load_dwordx2 v[26:27], v7, s[60:61] sc1\n"
    "s_waitcnt vmcnt(0)\n"
    "v_cmp_ne_u64 vcc, s[34:35], v[26:27]\n"
    "s_cbranch_vccz .Lk_c64\n"
    "s_sub_u32 s45, s45, 1\n"
    "s_cbranch_scc0 .Lk_l65\n"
    "s_branch .Lk_c64\n"
    ".Lk_r68:\n"
    "s_mov_b32 s45, 131072\n"
    ".Lk_l70:\n"
    "s_sleep 3\n"
    "global_load_dwordx2 v[20:21], v8, s[66:67] sc1\n"
    "s_waitcnt vmcnt(0)\n"
    "v_cmp_ne_u64 vcc, s[34:35], v[20:21]\n"
    "s_cbranch_vccz .Lk_c69\n"
    "s_sub_u32 s45, s45, 1\n"
    "s_cbranch_scc0 .Lk_l70\n"
    "s_branch .Lk_c69\n"
    ".Lk_r71:\n"
    "s_mov_b32 s45, 131072\n"
    ".Lk_l73:\n"
    "s_sleep 1\n"
    "global_load_dwordx4 v[240:243], v18, s[50:51] offset:0 sc1\n"
    "global_load_dwordx4 v[244:247], v18, s[50:51] offset:16 sc1\n"
    "s_waitcnt vmcnt(0)\n"
    "v_bitop3_b32 v241, v241, v243, s34 bitop3:0x7e\n"
    "v_bitop3_b32 v245, v245, v247, s34 bitop3:0x7e\n"
    "v_or_b32 v241, v241, v245\n"
    "v_cmp_ne_u32 vcc, 0, v241\n"
    "s_cbranch_vccz .Lk_c72\n"
    "s_sub_u32 s45, s45, 1\n"
    "s_cbranch_scc0 .Lk_l73\n"
    "s_branch .Lk_c72\n"
    ".Lk_r81:\n"
    "s_mov_b32 s45, 131072\n"
    ".Lk_l83:\n"
    "s_sleep 1\n"
    "global_load_dwordx4 v[240:243], v18, s[50:51] offset:0 sc1\n"
    "global_load_dwordx4 v[244:247], v18, s[50:51] offset:16 sc1\n"
    "s_waitcnt vmcnt(0)\n"
    "v_bitop3_b32 v241, v241, v243, s34 bitop3:0x7e\n"
    "v_bitop3_b32 v245, v245, v247, s34 bitop3:0x7e\n"
    "v_or_b32 v241, v241, v245\n"
    "v_cmp_ne_u32 vcc, 0, v241\n"
    "s_cbranch_vccz .Lk_c82\n"
    "s_sub_u32 s45, s45, 1\n"
    "s_cbranch_scc0 .Lk_l83\n"
    "s_branch .Lk_c82\n"
    ".Lk_r84:\n"
    "s_mov_b32 s45, 131072\n"
    ".Lk_l86:\n"
    "s_sleep 3\n"
    "global_load_dwordx2 v[20:21], v9, s[60:61] sc1\n"
    "s_waitcnt vmcnt(0)\n"
    "v_cmp_ne_u64 vcc, s[34:35], v[20:21]\n"
    "s_cbranch_vccz .Lk_c85\n"
    "s_sub_u32 s45, s45, 1\n"
    "s_cbranch_scc0 .Lk_l86\n"
    "s_branch .Lk_c85\n"
    ".Lk_r87:\n"
    "s_mov_b32 s45, 131072\n"
    ".Lk_l89:\n"
    "s_sleep 3\n"
    "global_load_dwordx2 v[26:27], v7, s[60:61] sc1\n"
    "s_waitcnt vmcnt(0)\n"
    "v_cmp_ne_u64 vcc, s[34:35], v[26:27]\n"
    "s_cbranch_vccz .Lk_c88\n"
    "s_sub_u32 s45, s45, 1\n"
    "s_cbranch_scc0 .Lk_l89\n"
    "s_branch .Lk_c88\n"
    ".Lk_r96:\n"
    "s_mov_b32 s45, 131072\n"
    ".Lk_l98:\n"
    "s_sleep 1\n"
    "global_load_dwordx2 v[46:47], v8, s[10:11] sc1\n"
    "s_waitcnt vmcnt(0)\n"
    "v_cmp_ne_u32 vcc, s34, v47\n"
    "s_cbranch_vccz .Lk_c97\n"
    "s_sub_u32 s45, s45, 1\n"
    "s_cbranch_scc0 .Lk_l98\n"
    "s_branch .Lk_c97\n"
    ".Lk_r99:\n"
    "s_mov_b32 s45, 131072\n"
    ".Lk_l101:\n"
    "s_sleep 3\n"
    "global_load_dwordx2 v[40:41], v7, s[48:49] sc1\n"
    "s_waitcnt vmcnt(0)\n"
    "v_cmp_ne_u64 vcc, s[34:35], v[40:41]\n"
    "s_cbranch_vccz .Lk_c100\n"
    "s_sub_u32 s45, s45, 1\n"
    "s_cbranch_scc0 .Lk_l101\n"
    "s_branch .Lk_c100\n"
    ".Lk_r102:\n"
    "s_mov_b32 s45, 131072\n"
    ".Lk_l104:\n"
    "s_sleep 1\n"
    "global_load_dwordx2 v[42:43], v8, s[50:51] sc1\n"
    "s_waitcnt vmcnt(0)\n"
    "v_cmp_ne_u32 vcc, s34, v43\n"
    "s_cbranch_vccz .Lk_c103\n"
    "s_sub_u32 s45, s45, 1\n"
    "s_cbranch_scc0 .Lk_l104\n"
    "s_branch .Lk_c103\n"
    ".Lk_r105:\n"
    "s_mov_b32 s45, 131072\n"
    ".Lk_l107:\n"
    "s_sleep 3\n"
    "global_load_dwordx2 v[40:41], v7, s[48:49] sc1\n"
    "s_waitcnt vmcnt(0)\n"
    "v_cmp_ne_u64 vcc, s[34:35], v[40:41]\n"
    "s_cbranch_vccz .Lk_c106\n"
    "s_sub_u32 s45, s45, 1\n"
    "s_cbranch_scc0 .Lk_l107\n"
    "s_branch .Lk_c106\n"
    ".Lk_r108:\n"
    "s_mov_b32 s45, 131072\n"
    ".Lk_l110:\n"
    "s_sleep 1\n"
    "global_load_dwordx2 v[42:43], v8, s[50:51] sc1\n"
    "s_waitcnt vmcnt(0)\n"
    "v_cmp_ne_u32 vcc, s34, v43\n"
    "s_cbranch_vccz .Lk_c109\n"
    "s_sub_u32 s45, s45, 1\n"
    "s_cbranch_scc0 .Lk_l110\n"
    "s_branch .Lk_c109\n"
    ".Lk_r116:\n"
    "s_mov_b32 s45, 131072\n"
    ".Lk_l118:\n"
    "s_sleep 1\n"
    "global_load_dwordx2 v[42:43], v8, s[50:51] sc1\n"
    "s_waitcnt vmcnt(0)\n"
    "v_cmp_ne_u32 vcc, s34, v43\n"
    "s_cbranch_vccz .Lk_c117\n"
    "s_sub_u32 s45, s45, 1\n"
    "s_cbranch_scc0 .Lk_l118\n"
    "s_branch .Lk_c117\n"
    ".Lk_r121:\n"
    "s_mov_b32 s45, 131072\n"
    ".Lk_l123:\n"
    "s_sleep 3\n"
    "global_load_dwordx2 v[40:41], v30, s[52:53] sc1\n"
    "s_waitcnt vmcnt(0)\n"
    "v_cmp_ne_u64 vcc, s[34:35], v[40:41]\n"
    "s_cbranch_vccz .Lk_c122\n"
    "s_sub_u32 s45, s45, 1\n"
    "s_cbranch_scc0 .Lk_l123\n"
    "s_branch .Lk_c122\n"
    ".Lk_r125:\n"
    "s_mov_b32 s45, 131072\n"
    ".Lk_l127:\n"
    "s_sleep 1\n"
    "global_load_dwordx2 v[42:43], v8, s[50:51] sc1\n"
    "s_waitcnt vmcnt(0)\n"
    "v_cmp_ne_u32 vcc, s34, v43\n"
    "s_cbranch_vccz .Lk_c126\n"
    "s_sub_u32 s45, s45, 1\n"
    "s_cbranch_scc0 .Lk_l127\n"
    "s_branch .Lk_c126\n"
    ".Lk_r143:\n"
    "s_mov_b32 s45, 131072\n"
    ".Lk_l145:\n"
    "s_sleep 3\n"
    "global_load_dwordx2 v[40:41], v30, s[52:53] sc1\n"
    "s_waitcnt vmcnt(0)\n"
    "v_cmp_ne_u64 vcc, s[34:35], v[40:41]\n"
    "s_cbranch_vccz .Lk_c144\n"
    "s_sub_u32 s45, s45, 1\n"
    "s_cbranch_scc0 .Lk_l145\n"
    "s_branch .Lk_c144\n"
    ".Lk_r161:\n"
    "s_mov_b32 s45, 131072\n"
    ".Lk_l163:\n"
    "s_sleep 3\n"
    "global_load_dwordx2 v[42:43], v30, s[52:53] sc1\n"
    "s_waitcnt vmcnt(0)\n"
    "v_cmp_ne_u64 vcc, s[34:35], v[42:43]\n"
    "s_cbranch_vccz .Lk_c162\n"
    "s_sub_u32 s45, s45, 1\n"
    "s_cbranch_scc0 .Lk_l163\n"
    "s_branch .Lk_c162\n"
    ".Lk_r178:\n"
    "s_mov_b32 s45, 131072\n"
    ".Lk_l180:\n"
    "s_sleep 3\n"
    "global_load_dwordx2 v[40:41], v30, s[52:53] sc1\n"
    "s_waitcnt vmcnt(0)\n"
    "v_cmp_ne_u64 vcc, s[34:35], v[40:41]\n"
    "s_cbranch_vccz .Lk_c179\n"
    "s_sub_u32 s45, s45, 1\n"
    "s_cbranch_scc0 .Lk_l180\n"
    "s_branch .Lk_c179\n"
    ".Lk_r191:\n"
    "s_mov_b32 s45, 131072\n"
    ".Lk_l193:\n"
    "s_sleep 8\n"
    "global_load_dwordx2 v[40:41], v18, s[48:49] sc1\n"
    "s_waitcnt vmcnt(0)\n"
    "v_cmp_ne_u64 vcc, s[34:35], v[40:41]\n"
    "s_cbranch_vccz .Lk_c192\n"
    "s_sub_u32 s45, s45, 1\n"
    "s_cbranch_scc0 .Lk_l193\n"
    "s_branch .Lk_c192\n"
    ".Lk_r194:\n"
    "s_mov_b32 s45, 131072\n"
    ".Lk_l196:\n"
    "s_sleep 4\n"
    "global_load_dwordx2 v[40:41], v3, s[52:53] sc1\n"
    "s_waitcnt vmcnt(0)\n"
    "v_cmp_ne_u64 vcc, s[34:35], v[40:41]\n"
    "s_cbranch_vccz .Lk_c195\n"
    "s_sub_u32 s45, s45, 1\n"
    "s_cbranch_scc0 .Lk_l196\n"
    "s_branch .Lk_c195\n"
    ".Lk_r197:\n"
    "s_mov_b32 s45, 131072\n"
    ".Lk_l199:\n"
    "s_sleep 8\n"
    "global_load_dwordx2 v[40:41], v7, s[78:79] sc1\n"
    "s_waitcnt vmcnt(0)\n"
    "v_cmp_ne_u64 vcc, s[34:35], v[40:41]\n"
    "s_cbranch_vccz .Lk_c198\n"
    "s_sub_u32 s45, s45, 1\n"
    "s_cbranch_scc0 .Lk_l199\n"
    "s_branch .Lk_c198\n"
    ".Lk_r202:\n"
    "s_mov_b32 s45, 131072\n"
    ".Lk_l204:\n"
    "s_sleep 4\n"
    "global_load_dwordx2 v[40:41], v3, s[52:53] sc1\n"
    "s_waitcnt vmcnt(0)\n"
    "v_cmp_ne_u64 vcc, s[34:35], v[40:41]\n"
    "s_cbranch_vccz .Lk_c203\n"
    "s_sub_u32 s45, s45, 1\n"
    "s_cbranch_scc0 .Lk_l204\n"
    "s_branch .Lk_c203\n"
    ".Lk_end:\n"
    : "+v"(tid)
    : "s"(__builtin_amdgcn_kernarg_segment_ptr()), "s"(blockIdx.x), "n"(0), "s"((unsigned)(size_t)(__attribute__((address_space(3))) char*)smem)
    : "v1", "v2", "v3", "v4", "v5", "v6", "v7", "v8", "v9", "v10", "v11", "v12", "v13", "v14", "v15", "v16", "v17", "v18", "v19", "v20", "v21", "v22", "v23", "v24", "v25", "v26", "v27", "v28", "v29", "v30", "v31", "v32", "v33", "v34", "v35", "v36", "v37", "v38", "v39", "v40", "v41", "v42", "v43", "v44", "v45", "v46", "v47", "v48", "v49", "v50", "v51", "v52", "v53", "v54", "v55", "v56", "v57", "v58", "v59", "v60", "v61", "v62", "v63", "v64", "v65", "v66", "v67", "v68", "v69", "v70", "v71", "v72", "v73", "v74", "v75", "v76", "v77", "v78", "v79", "v80", "v81", "v82", "v83", "v84", "v85", "v86", "v87", "v88", "v89", "v90", "v91", "v92", "v93", "v94", "v95", "v96", "v97", "v98", "v99", "v100", "v101", "v102", "v103", "v104", "v105", "v106", "v107", "v108", "v109", "v110", "v111", "v112", "v113", "v114", "v115", "v116", "v117", "v118", "v119", "v120", "v121", "v122", "v123", "v124", "v125", "v126", "v127", "v128", "v129", "v130", "v131", "v132", "v133", "v134", "v135", "v136", "v137", "v138", "v139", "v140", "v141", "v142", "v143", "v144", "v145", "v146", "v147", "v148", "v149", "v150", "v151", "v152", "v153", "v154", "v155", "v156", "v157", "v158", "v159", "v160", "v161", "v162", "v163", "v164", "v165", "v166", "v167", "v168", "v169", "v170", "v171", "v172", "v173", "v174", "v175", "v176", "v177", "v178", "v179", "v180", "v181", "v182", "v183", "v184", "v185", "v186", "v187", "v188", "v189", "v190", "v191", "v192", "v193", "v194", "v195", "v196", "v197", "v198", "v199", "v200", "v201", "v202", "v203", "v204", "v205", "v206", "v207", "v208", "v209", "v210", "v211", "v212", "v213", "v214", "v215", "v216", "v217", "v218", "v219", "v220", "v221", "v222", "v223", "v224", "v225", "v226", "v227", "v228", "v229", "v230", "v231", "v232", "v233", "v234", "v235", "v236", "v237", "v238", "v239", "v240", "v241", "v242", "v243", "v244", "v245", "v246", "v247", "v248", "v249", "v250", "v251", "v252", "v253", "v254", "v255", "s4", "s5", "s6", "s7", "s8", "s9", "s10", "s11", "s12", "s13", "s14", "s15", "s16", "s17", "s18", "s19", "s20", "s21", "s22", "s23", "s24", "s25", "s26", "s27", "s28", "s29", "s30", "s31", "s34", "s35", "s36", "s37", "s38", "s39", "s40", "s41", "s42", "s43", "s44", "s45", "s46", "s47", "s48", "s49", "s50", "s51", "s52", "s53", "s54", "s55", "s56", "s57", "s58", "s59", "s60", "s61", "s62", "s63", "s64", "s65", "s66", "s67", "s68", "s69", "s70", "s71", "s72", "s73", "s74", "s75", "s76", "s77", "s78", "s79", "s80", "s81", "s82", "s83", "s84", "s85", "s86", "s87", "s88", "s89", "s90", "s91", "s92", "s93", "s94", "s95", "s96", "s97", "s98", "s99", "vcc", "scc", "memory");
}

extern "C" void kernel_launch(void* const* d_in, const int* in_sizes, int n_in, void* d_out, int out_size, void* d_ws, size_t ws_size,
                              hipStream_t stream) {
  k_fused<<<256, 512, 0, stream>>>(d_in[0], d_in[1], d_in[2], d_in[3], d_in[4], d_in[5], d_in[7], d_in[8], d_in[9], (char*)d_ws + WS_OFF, d_out);
}
```
